```python
import torch

def _softmax(x, axis=-1):
    return torch.softmax(x, dim=axis)

def _gelu(x):
    return torch.nn.functional.gelu(x, approximate='tanh')

def _silu(x):
    return torch.nn.functional.silu(x)

def _sigmoid(x):
    return torch.sigmoid(x)

def _layernorm(x, axis=-1, eps=1e-05):
    m = x.mean(dim=axis, keepdim=True)
    v = x.var(dim=axis, keepdim=True, unbiased=False)
    return (x - m) / torch.sqrt(v + eps)

def _scatter_add(tgt, idx, src, axis=0):
    return tgt.clone().index_add_(axis, idx, src)

def _segment_sum(src, seg, n):
    out = torch.zeros((n,) + src.shape[1:], dtype=src.dtype)
    return out.index_add_(0, seg, src)

def _pad(x, pw):
    flat = tuple((v for pair in reversed(pw) for v in pair))
    return torch.nn.functional.pad(x, flat)
kernel_name = 'adaLN_cond__linear_kernel_hedgehog__glu_gated_geglu__prompt'

def setup_inputs():

    _w = lambda a: (lambda t: t.to(torch.bfloat16) if t.is_floating_point() else t)(torch.as_tensor(a))

    def _A_setup():
        B, S, D, H = (1, 2048, 512, 32)
        Dh = D // H
        return {'x': _w((lambda *s: torch.randn(*s, dtype=torch.bfloat16) * 0.1)(B, S, D)), 'wqkv': _w((lambda *s: torch.randn(*s, dtype=torch.bfloat16) * 0.2)(D, 3 * D)), 'w_phi': _w((lambda *s: torch.randn(*s, dtype=torch.bfloat16) * 0.2)(H, Dh, Dh) * Dh ** (-0.5)), 'wo': _w((lambda *s: torch.randn(*s, dtype=torch.bfloat16) * 0.2)(D, D))}
    _A_in = _A_setup()

    def _B_setup():
        B, S, Dm, Df = (1, 2048, 512, 1376)
        return {'x': _w((lambda *s: torch.randn(*s, dtype=torch.bfloat16) * 0.1)(B, S, Dm)), 'w_gate': _w((lambda *s: torch.randn(*s, dtype=torch.bfloat16) * 3.2)(Dm, Df) * Dm ** (-0.5)), 'w_up': _w((lambda *s: torch.randn(*s, dtype=torch.bfloat16) * 3.2)(Dm, Df) * Dm ** (-0.5)), 'w_down': _w((lambda *s: torch.randn(*s, dtype=torch.bfloat16) * 3.2)(Df, Dm) * Df ** (-0.5))}
    _B_in = _B_setup()
    return {'A_x': _A_in['x'], 'A_wqkv': _A_in['wqkv'], 'A_w_phi': _A_in['w_phi'], 'A_wo': _A_in['wo'], 'B_x': _B_in['x'], 'B_w_gate': _B_in['w_gate'], 'B_w_up': _B_in['w_up'], 'B_w_down': _B_in['w_down'], 'c': _w((lambda *s: torch.randn(*s, dtype=torch.bfloat16) * 0.4)(1, 512) * 8.0), 'w_mod': _w((lambda *s: torch.randn(*s, dtype=torch.bfloat16) * 0.4)(512, 6 * 512) * 512 ** (-0.5) * 8.0)}

def reference(A_x, A_wqkv, A_w_phi, A_wo, B_x, B_w_gate, B_w_up, B_w_down, c, w_mod):

    def _A_ref(x, wqkv, w_phi, wo):
        B, S, D = x.shape
        H = 32
        Dh = D // H
        qkv = (lambda x, *d: x.permute(*d))((x @ wqkv).reshape(B, S, 3, H, Dh), 2, 0, 3, 1, 4)
        q, k, v = (qkv[0], qkv[1], qkv[2])
        pq = torch.einsum('bhsd,hdf->bhsf', q, w_phi)
        pk = torch.einsum('bhsd,hdf->bhsf', k, w_phi)
        phi_q = torch.concatenate([_softmax(pq, axis=-1), _softmax(-pq, axis=-1)], axis=-1)
        phi_k = torch.concatenate([_softmax(pk, axis=-1), _softmax(-pk, axis=-1)], axis=-1)
        kv = torch.einsum('bhsf,bhsd->bhfd', phi_k, v)
        z = torch.einsum('bhsf->bhf', phi_k)
        num = torch.einsum('bhqf,bhfd->bhqd', phi_q, kv)
        den = torch.einsum('bhqf,bhf->bhq', phi_q, z)[..., None] + 1e-06
        o = num / den
        return (lambda x, *d: x.permute(*d))(o, 0, 2, 1, 3).reshape(B, S, D) @ wo

    def _B_ref(x, w_gate, w_up, w_down):
        g = torch.einsum('bsd,df->bsf', x, w_gate)
        u = torch.einsum('bsd,df->bsf', x, w_up)
        h = _gelu(g) * u
        return torch.einsum('bsf,fd->bsd', h, w_down)
    x = A_x
    mod = (_silu(c) @ w_mod).reshape(c.shape[0], 6, 1, -1)
    sa, ca, ga, sb, cb, gb = (mod[:, 0].clone(), mod[:, 1].clone(), mod[:, 2].clone(), mod[:, 3].clone(), mod[:, 4].clone(), mod[:, 5].clone())
    h = _layernorm(x, axis=-1) * (1 + ca) + sa
    x = x + ga * _A_ref(h, A_wqkv, A_w_phi, A_wo)
    h = _layernorm(x, axis=-1) * (1 + cb) + sb
    x = x + gb * _B_ref(h, B_w_gate, B_w_up, B_w_down)
    return x
```

```cpp
#include <hip/hip_runtime.h>
#include <hip/hip_bf16.h>
typedef __hip_bfloat16 bf16;
__device__ __forceinline__ float rb(float x){ return __bfloat162float(__float2bfloat16(x)); }
__device__ __forceinline__ float ld(const bf16* p, long i){ return __bfloat162float(p[i]); }
__global__ void k_mod(const bf16* c, const bf16* wm, float* mod){
  int j = blockIdx.x*256+threadIdx.x; float acc=0;
  for(int k=0;k<512;++k){ float cv=ld(c,k); float s=rb(cv/(1.f+__expf(-cv))); acc+=s*ld(wm,(long)k*3072+j);}
  mod[j]=rb(acc);
}
__global__ void k_ln(const bf16* x, const float* shift, const float* scale, bf16* h){
  int r=blockIdx.x, t=threadIdx.x; __shared__ float red[256];
  float a=ld(x,(long)r*512+t), b=ld(x,(long)r*512+256+t);
  red[t]=a+b; __syncthreads(); for(int s=128;s>0;s>>=1){ if(t<s) red[t]+=red[t+s]; __syncthreads(); }
  float mean=red[0]/512.f; __syncthreads();
  red[t]=(a-mean)*(a-mean)+(b-mean)*(b-mean); __syncthreads(); for(int s=128;s>0;s>>=1){ if(t<s) red[t]+=red[t+s]; __syncthreads(); }
  float var=red[0]/512.f; float mb=rb(mean), vb=rb(var); float den=rb(sqrtf(rb(vb+1e-5f)));
  float xa=rb(rb(a-mb)/den), xb=rb(rb(b-mb)/den);
  h[(long)r*512+t]=__float2bfloat16(rb(xa*rb(1.f+scale[t]))+shift[t]);
  h[(long)r*512+256+t]=__float2bfloat16(rb(xb*rb(1.f+scale[256+t]))+shift[256+t]);
}
__global__ void k_gemm(const bf16* A, const bf16* B, bf16* C, int M, int N, int K){
  int n=blockIdx.x*64+threadIdx.x, m0=blockIdx.y*8; if(n>=N) return;
  float acc[8]={0,0,0,0,0,0,0,0};
  for(int k=0;k<K;++k){ float b=ld(B,(long)k*N+n);
    #pragma unroll
    for(int i=0;i<8;++i) acc[i]+=ld(A,(long)(m0+i)*K+k)*b; }
  for(int i=0;i<8;++i) C[(long)(m0+i)*N+n]=__float2bfloat16(acc[i]);
}
__global__ void k_phi(const bf16* qkv, const bf16* wphi, bf16* phi  ){
  int idx=blockIdx.x*256+threadIdx.x; int which=idx/(2048*32); int s=(idx/32)%2048, h=idx%32;
  float p[16]; float mx=-1e30f, mn=1e30f;
  for(int f=0;f<16;++f){ float a=0; for(int d=0;d<16;++d) a+=ld(qkv,(long)s*1536+which*512+h*16+d)*ld(wphi,h*256+d*16+f); p[f]=rb(a); mx=fmaxf(mx,p[f]); mn=fminf(mn,p[f]); }
  float sp=0,sn=0; for(int f=0;f<16;++f){ sp+=__expf(p[f]-mx); sn+=__expf(mn-p[f]); }
  bf16* o=phi+((long)(which*2048+s)*32+h)*32;
  for(int f=0;f<16;++f){ o[f]=__float2bfloat16(__expf(p[f]-mx)/sp); o[16+f]=__float2bfloat16(__expf(mn-p[f])/sn); }
}
__global__ void k_kv(const bf16* qkv, const bf16* phi, float* kv  ){
  int h=blockIdx.x, t=threadIdx.x; if(t>=32*17) return; int f=t/17, d=t%17; float a=0;
  for(int s=0;s<2048;++s){ float p=ld(phi,((long)(2048+s)*32+h)*32+f); a+= p*(d<16? ld(qkv,(long)s*1536+1024+h*16+d):1.f);}
  kv[h*544+t]=rb(a);
}
__global__ void k_o(const bf16* phi, const float* kv, bf16* o){
  int idx=blockIdx.x*256+threadIdx.x; int s=idx/512, h=(idx/16)%32, d=idx%16; float num=0,den=0;
  for(int f=0;f<32;++f){ float p=ld(phi,((long)s*32+h)*32+f); num+=p*kv[h*544+f*17+d]; den+=p*kv[h*544+f*17+16]; }
  o[idx]=__float2bfloat16(rb(num)/rb(rb(den)+1e-6f));
}
__global__ void k_res(const bf16* x, const bf16* y, const float* g, bf16* out){
  int idx=blockIdx.x*256+threadIdx.x; out[idx]=__float2bfloat16(ld(x,idx)+rb(g[idx%512]*ld(y,idx)));
}
__global__ void k_geglu(const bf16* g, const bf16* u, bf16* hh){
  int idx=blockIdx.x*256+threadIdx.x; float x=ld(g,idx); float t=tanhf(0.7978845608028654f*(x+0.044715f*x*x*x)); float ge=rb(0.5f*x*(1.f+t));
  hh[idx]=__float2bfloat16(ge*ld(u,idx));
}
extern "C" void kernel_launch(void* const* d_in, const int* in_sizes, int n_in, void* d_out, int out_size, void* d_ws, size_t ws_size, hipStream_t stream){
  const bf16* x=(const bf16*)d_in[0]; const bf16* wqkv=(const bf16*)d_in[1]; const bf16* wphi=(const bf16*)d_in[2]; const bf16* wo=(const bf16*)d_in[3];
  const bf16* wg=(const bf16*)d_in[5]; const bf16* wu=(const bf16*)d_in[6]; const bf16* wd=(const bf16*)d_in[7]; const bf16* c=(const bf16*)d_in[8]; const bf16* wm=(const bf16*)d_in[9];
  char* ws=(char*)d_ws; size_t MB=1<<20;
  float* mod=(float*)ws; bf16* h=(bf16*)(ws+1*MB); bf16* qkv=(bf16*)(ws+4*MB); bf16* phi=(bf16*)(ws+12*MB); float* kv=(float*)(ws+24*MB); bf16* o=(bf16*)(ws+25*MB);
  bf16* ao=(bf16*)(ws+28*MB); bf16* x1=(bf16*)(ws+32*MB); bf16* h2=(bf16*)(ws+36*MB); bf16* g=(bf16*)(ws+40*MB); bf16* u=(bf16*)(ws+48*MB); bf16* hh=(bf16*)(ws+56*MB); bf16* dn=(bf16*)(ws+64*MB);
  k_mod<<<12,256,0,stream>>>(c,wm,mod);
  k_ln<<<2048,256,0,stream>>>(x,mod,mod+512,h);
  k_gemm<<<dim3(24,256),64,0,stream>>>(h,wqkv,qkv,2048,1536,512);
  k_phi<<<512,256,0,stream>>>(qkv,wphi,phi);
  k_kv<<<32,576,0,stream>>>(qkv,phi,kv);
  k_o<<<4096,256,0,stream>>>(phi,kv,o);
  k_gemm<<<dim3(8,256),64,0,stream>>>(o,wo,ao,2048,512,512);
  k_res<<<4096,256,0,stream>>>(x,ao,mod+1024,x1);
  k_ln<<<2048,256,0,stream>>>(x1,mod+1536,mod+2048,h2);
  k_gemm<<<dim3(22,256),64,0,stream>>>(h2,wg,g,2048,1376,512);
  k_gemm<<<dim3(22,256),64,0,stream>>>(h2,wu,u,2048,1376,512);
  k_geglu<<<2048*1376/256,256,0,stream>>>(g,u,hh);
  k_gemm<<<dim3(8,256),64,0,stream>>>(hh,wd,dn,2048,512,1376);
  k_res<<<4096,256,0,stream>>>(x1,dn,mod+2560,(bf16*)d_out);
}
```

```cpp
#include <hip/hip_runtime.h>
#include <hip/hip_bf16.h>
#include <stdint.h>

typedef __attribute__((ext_vector_type(8))) short bf16x8;
typedef __attribute__((ext_vector_type(4))) short bf16x4;
typedef __attribute__((ext_vector_type(4))) float f32x4;
typedef __attribute__((ext_vector_type(2))) float f32x2;
typedef __attribute__((ext_vector_type(2))) __bf16 bf2_t;
typedef __attribute__((ext_vector_type(4))) unsigned u32x4;
typedef __attribute__((ext_vector_type(2))) unsigned u32x2;
#define LAS __attribute__((address_space(3)))
#define DEVI __device__ __forceinline__

constexpr int S_ = 2048, D_ = 512, DF_ = 1376;
constexpr size_t WS_MOD = 0;
constexpr size_t WS_XN = 1 << 20;
constexpr size_t WS_PHIQ = 4 << 20;
constexpr size_t WS_KVP = 8 << 20;
constexpr size_t WS_O = 12 << 20;
constexpr size_t WS_X1 = 16 << 20;
constexpr size_t WS_ST2 = 20 << 20;
constexpr size_t WS_HH = 24 << 20;

DEVI unsigned pack2(float a, float b) { f32x2 p = {a, b}; bf2_t q = __builtin_convertvector(p, bf2_t); return __builtin_bit_cast(unsigned, q); }
DEVI float lo16(unsigned u) { return __uint_as_float(u << 16); }
DEVI float hi16(unsigned u) { return __uint_as_float(u & 0xffff0000u); }
DEVI float bf2f(unsigned short u) { return __uint_as_float(((unsigned)u) << 16); }
DEVI float xmax16(float v) { auto r = __builtin_amdgcn_permlane16_swap(__float_as_uint(v), __float_as_uint(v), false, false); return fmaxf(__uint_as_float(r[0]), __uint_as_float(r[1])); }
DEVI float xmax32(float v) { auto r = __builtin_amdgcn_permlane32_swap(__float_as_uint(v), __float_as_uint(v), false, false); return fmaxf(__uint_as_float(r[0]), __uint_as_float(r[1])); }
DEVI float xmin16(float v) { auto r = __builtin_amdgcn_permlane16_swap(__float_as_uint(v), __float_as_uint(v), false, false); return fminf(__uint_as_float(r[0]), __uint_as_float(r[1])); }
DEVI float xmin32(float v) { auto r = __builtin_amdgcn_permlane32_swap(__float_as_uint(v), __float_as_uint(v), false, false); return fminf(__uint_as_float(r[0]), __uint_as_float(r[1])); }
DEVI float xsum16(float v) { auto r = __builtin_amdgcn_permlane16_swap(__float_as_uint(v), __float_as_uint(v), false, false); return __uint_as_float(r[0]) + __uint_as_float(r[1]); }
DEVI float xsum32(float v) { auto r = __builtin_amdgcn_permlane32_swap(__float_as_uint(v), __float_as_uint(v), false, false); return __uint_as_float(r[0]) + __uint_as_float(r[1]); }
DEVI f32x4 mfma32(bf16x8 a, bf16x8 b, f32x4 c) { return __builtin_amdgcn_mfma_f32_16x16x32_bf16(a, b, c, 0, 0, 0); }
DEVI f32x4 mfma16(bf16x4 a, bf16x4 b, f32x4 c) { return __builtin_amdgcn_mfma_f32_16x16x16bf16_1k(a, b, c, 0, 0, 0); }
DEVI bf16x4 pack4(f32x4 v) { u32x2 u = {pack2(v[0], v[1]), pack2(v[2], v[3])}; return __builtin_bit_cast(bf16x4, u); }
DEVI bf16x8 cat8(bf16x4 a, bf16x4 b) { return __builtin_shufflevector(a, b, 0, 1, 2, 3, 4, 5, 6, 7); }
DEVI void glds16(const void* g, char* lds) { __builtin_amdgcn_global_load_lds((const unsigned*)g, (LAS unsigned*)lds, 16, 0, 0); }
DEVI bf16x4 trread(const char* lds) { return __builtin_amdgcn_ds_read_tr16_b64_v4i16((LAS bf16x4*)lds); }
DEVI bf16x8 trfrag(const char* lds, int step) { return cat8(trread(lds), trread(lds + step)); }
#define WAITV(n) asm volatile("s_waitcnt vmcnt(" #n ")" ::: "memory")
#define BAR() do { asm volatile("s_waitcnt lgkmcnt(0)" ::: "memory"); __builtin_amdgcn_s_barrier(); } while (0)

__global__ __launch_bounds__(64) void k0_mod_ln(const unsigned short* __restrict__ x, const unsigned short* __restrict__ c,
                                                const unsigned short* __restrict__ wmod, float* __restrict__ mod, char* __restrict__ xn) {
  const int lane = threadIdx.x;
  const int b = blockIdx.x;
  if (b < 192) {
    const int cb = (b & 7) * 24 + (b >> 3);
    const int half = lane & 1, rg = lane >> 1;
    const u32x4 c0 = *(const u32x4*)(c + rg * 16), c1 = *(const u32x4*)(c + rg * 16 + 8);
    u32x4 w[16];
#pragma unroll
    for (int i = 0; i < 16; ++i) w[i] = *(const u32x4*)(wmod + (size_t)(rg * 16 + i) * 3072 + cb * 16 + half * 8);
    float acc[8] = {0, 0, 0, 0, 0, 0, 0, 0};
#pragma unroll
    for (int i = 0; i < 16; ++i) {
      unsigned cu = (i < 8) ? c0[i >> 1] : c1[(i - 8) >> 1];
      float cv = (i & 1) ? hi16(cu) : lo16(cu);
      float s = cv / (1.f + __expf(-cv));
#pragma unroll
      for (int j = 0; j < 4; ++j) { acc[2 * j] += s * lo16(w[i][j]); acc[2 * j + 1] += s * hi16(w[i][j]); }
    }
#pragma unroll
    for (int j = 0; j < 8; ++j) {
      float v = acc[j];
      v += __shfl_xor(v, 2); v += __shfl_xor(v, 4); v += __shfl_xor(v, 8); v = xsum16(v); v = xsum32(v);
      acc[j] = v;
    }
    if (lane < 2) {
      const int col = cb * 16 + half * 8;
      const float add = ((col >= 512 && col < 1024) || (col >= 2048 && col < 2560)) ? 1.f : 0.f;
      f32x4 o0 = {acc[0] + add, acc[1] + add, acc[2] + add, acc[3] + add}, o1 = {acc[4] + add, acc[5] + add, acc[6] + add, acc[7] + add};
      *(f32x4*)(mod + col) = o0; *(f32x4*)(mod + col + 4) = o1;
    }
  } else {
    const int mt = b - 192;
    const int m = lane & 15, gq = lane >> 4;
    const unsigned short* xr = x + (size_t)(mt * 16 + m) * 512 + gq * 8;
    u32x4 v[16];
#pragma unroll
    for (int ks = 0; ks < 16; ++ks) v[ks] = *(const u32x4*)(xr + ks * 32);
    float s = 0;
#pragma unroll
    for (int ks = 0; ks < 16; ++ks)
#pragma unroll
      for (int j = 0; j < 4; ++j) s += lo16(v[ks][j]) + hi16(v[ks][j]);
    s = xsum32(xsum16(s));
    const float mean = s * (1.f / 512.f);
    float q = 0;
#pragma unroll
    for (int ks = 0; ks < 16; ++ks)
#pragma unroll
      for (int j = 0; j < 4; ++j) { float a = lo16(v[ks][j]) - mean, bb = hi16(v[ks][j]) - mean; q += a * a + bb * bb; }
    q = xsum32(xsum16(q));
    const float rstd = 1.f / sqrtf(q * (1.f / 512.f) + 1e-5f);
    const float nb = -mean * rstd;
#pragma unroll
    for (int ks = 0; ks < 16; ++ks) {
      u32x4 o;
#pragma unroll
      for (int j = 0; j < 4; ++j) o[j] = pack2(lo16(v[ks][j]) * rstd + nb, hi16(v[ks][j]) * rstd + nb);
      *(u32x4*)(xn + (size_t)(mt * 16 + ks) * 1024 + lane * 16) = o;
    }
  }
}

DEVI void featmap(f32x4 p, bf16x4& pos, bf16x4& neg) {
  float mx = fmaxf(fmaxf(p[0], p[1]), fmaxf(p[2], p[3]));
  float mn = fminf(fminf(p[0], p[1]), fminf(p[2], p[3]));
  mx = xmax32(xmax16(mx)); mn = xmin32(xmin16(mn));
  f32x4 ep, en; float sp = 0, sn = 0;
#pragma unroll
  for (int j = 0; j < 4; ++j) { ep[j] = __expf(p[j] - mx); en[j] = __expf(mn - p[j]); sp += ep[j]; sn += en[j]; }
  sp = xsum32(xsum16(sp)); sn = xsum32(xsum16(sn));
  const float ip = 1.f / sp, in = 1.f / sn;
#pragma unroll
  for (int j = 0; j < 4; ++j) { ep[j] *= ip; en[j] *= in; }
  pos = pack4(ep); neg = pack4(en);
}

__global__ __launch_bounds__(512, 2) void k1_qkv(const char* __restrict__ xn, const unsigned short* __restrict__ wqkv, const unsigned short* __restrict__ wphi,
                                                const float* __restrict__ mod, char* __restrict__ phiq, float* __restrict__ kvp) {
  extern __shared__ __attribute__((aligned(16))) char sm[];
  const int tid = threadIdx.x, lane = tid & 63, w = __builtin_amdgcn_readfirstlane(tid >> 6);
  const int b = blockIdx.x, xcd = b & 7, idx = b >> 3;
  const int r = (xcd >> 1) * 4 + (idx >> 3), g = (xcd & 1) * 8 + (idx & 7);
  const int wr = w & 3, kh = w >> 2;
  const int m = lane & 15, gq = lane >> 4;
  u32x4 xa[2][8];
#pragma unroll
  for (int mt = 0; mt < 2; ++mt)
#pragma unroll
    for (int ks = 0; ks < 8; ++ks) xa[mt][ks] = *(const u32x4*)(xn + (size_t)((r * 8 + wr * 2 + mt) * 16 + kh * 8 + ks) * 1024 + lane * 16);
  { f32x2 mv = *(const f32x2*)(mod + tid * 2); *(f32x2*)(sm + 98304 + tid * 8) = mv; }
  const int head = 2 * g + kh;
  bf16x4 wpT;
#pragma unroll
  for (int j = 0; j < 4; ++j) wpT[j] = (short)wphi[head * 256 + (4 * gq + j) * 16 + m];
#pragma unroll
  for (int i = 0; i < 12; ++i) {
    const int id = w * 12 + i, seg = id >> 5, rb = id & 31;
    const int row = rb * 16 + (lane >> 2);
    const int sc = (lane & 3) ^ (((row >> 3) & 1) << 1);
    glds16(wqkv + (size_t)row * 1536 + seg * 512 + g * 32 + sc * 8, sm + id * 1024);
  }
  __syncthreads();
  bf16x8 ha[2][8];
#pragma unroll
  for (int ks = 0; ks < 8; ++ks) {
    const int k0 = kh * 256 + ks * 32 + gq * 8;
    const f32x4 sa0 = *(const f32x4*)(sm + 98304 + k0 * 4), sa1 = *(const f32x4*)(sm + 98304 + k0 * 4 + 16);
    const f32x4 s0 = *(const f32x4*)(sm + 98304 + 2048 + k0 * 4), s1 = *(const f32x4*)(sm + 98304 + 2048 + k0 * 4 + 16);
#pragma unroll
    for (int mt = 0; mt < 2; ++mt) {
      u32x4 o;
      o[0] = pack2(lo16(xa[mt][ks][0]) * s0[0] + sa0[0], hi16(xa[mt][ks][0]) * s0[1] + sa0[1]);
      o[1] = pack2(lo16(xa[mt][ks][1]) * s0[2] + sa0[2], hi16(xa[mt][ks][1]) * s0[3] + sa0[3]);
      o[2] = pack2(lo16(xa[mt][ks][2]) * s1[0] + sa1[0], hi16(xa[mt][ks][2]) * s1[1] + sa1[1]);
      o[3] = pack2(lo16(xa[mt][ks][3]) * s1[2] + sa1[2], hi16(xa[mt][ks][3]) * s1[3] + sa1[3]);
      ha[mt][ks] = __builtin_bit_cast(bf16x8, o);
    }
  }
  WAITV(0);
  __syncthreads();
  f32x4 acc[3][2][2];
#pragma unroll
  for (int s = 0; s < 3; ++s)
#pragma unroll
    for (int hb = 0; hb < 2; ++hb)
#pragma unroll
      for (int mt = 0; mt < 2; ++mt) acc[s][hb][mt] = f32x4{0, 0, 0, 0};
  {
    const int q = (lane & 15) >> 2, p = lane & 3;
    const char* tb[2];
#pragma unroll
    for (int hb = 0; hb < 2; ++hb) tb[hb] = sm + (kh * 256 + gq * 8 + q) * 64 + (((hb * 2 + (p >> 1)) ^ ((gq & 1) << 1)) * 16) + (p & 1) * 8;
#pragma unroll
    for (int ks = 0; ks < 8; ++ks) {
#pragma unroll
      for (int s = 0; s < 3; ++s)
#pragma unroll
        for (int hb = 0; hb < 2; ++hb) {
          const bf16x8 wf = trfrag(tb[hb] + s * 32768 + ks * 2048, 256);
#pragma unroll
          for (int mt = 0; mt < 2; ++mt) {
            if (s < 2) acc[s][hb][mt] = mfma32(wf, ha[mt][ks], acc[s][hb][mt]);
            else acc[s][hb][mt] = mfma32(ha[mt][ks], wf, acc[s][hb][mt]);
          }
        }
    }
  }
  __syncthreads();
#pragma unroll
  for (int s = 0; s < 3; ++s)
#pragma unroll
    for (int mt = 0; mt < 2; ++mt) *(f32x4*)(sm + (w * 6 + s * 2 + mt) * 1024 + lane * 16) = kh ? acc[s][0][mt] : acc[s][1][mt];
  __syncthreads();
  f32x4 t[3][2];
#pragma unroll
  for (int s = 0; s < 3; ++s)
#pragma unroll
    for (int mt = 0; mt < 2; ++mt) {
      const f32x4 o = *(const f32x4*)(sm + ((w ^ 4) * 6 + s * 2 + mt) * 1024 + lane * 16);
      t[s][mt] = (kh ? acc[s][1][mt] : acc[s][0][mt]) + o;
    }
  const f32x4 z4 = {0, 0, 0, 0};
  bf16x4 ident;
#pragma unroll
  for (int j = 0; j < 4; ++j) ident[j] = (4 * gq + j == m) ? (short)0x3f80 : (short)0;
  bf16x4 pkp[2], pkn[2];
#pragma unroll
  for (int mt = 0; mt < 2; ++mt) {
    bf16x4 pos, neg;
    featmap(mfma16(wpT, pack4(t[0][mt]), z4), pos, neg);
    *(bf16x8*)(phiq + (size_t)((((r * 32 + head) * 4 + wr) * 2 + mt) * 64 + lane) * 16) = cat8(pos, neg);
    featmap(mfma16(wpT, pack4(t[1][mt]), z4), pos, neg);
    pkp[mt] = pack4(mfma16(pos, ident, z4));
    pkn[mt] = pack4(mfma16(neg, ident, z4));
  }
  const bf16x8 vf = cat8(pack4(t[2][0]), pack4(t[2][1]));
  const bf16x8 fp = cat8(pkp[0], pkp[1]), fn = cat8(pkn[0], pkn[1]);
  const bf16x8 ones = {0x3f80, 0x3f80, 0x3f80, 0x3f80, 0x3f80, 0x3f80, 0x3f80, 0x3f80};
  f32x4 part[4];
  part[0] = mfma32(fp, vf, z4); part[1] = mfma32(fn, vf, z4); part[2] = mfma32(fp, ones, z4); part[3] = mfma32(fn, ones, z4);
#pragma unroll
  for (int i = 0; i < 4; ++i) *(f32x4*)(sm + 49152 + (w * 4 + i) * 1024 + lane * 16) = part[i];
  __syncthreads();
  {
    f32x4 s = z4;
#pragma unroll
    for (int j = 0; j < 4; ++j) s += *(const f32x4*)(sm + 49152 + ((kh * 4 + j) * 4 + wr) * 1024 + lane * 16);
    *(f32x4*)(kvp + (size_t)(((r * 32 + head) * 4 + wr) * 64 + lane) * 4) = s;
  }
}

__global__ __launch_bounds__(512, 2) void k1b_o(const char* __restrict__ phiq, const float* __restrict__ kvp, char* __restrict__ o) {
  __shared__ __attribute__((aligned(16))) char sm[8192];
  const int tid = threadIdx.x, lane = tid & 63, w = __builtin_amdgcn_readfirstlane(tid >> 6);
  const int b = blockIdx.x, xcd = b & 7, idx = b >> 3;
  const int r = (xcd >> 1) * 4 + (idx >> 3), g = (xcd & 1) * 8 + (idx & 7);
  const int wr = w & 3, kh = w >> 2, head = 2 * g + kh;
  const int m = lane & 15, gq = lane >> 4;
  bf16x8 pq[2];
#pragma unroll
  for (int mt = 0; mt < 2; ++mt) pq[mt] = *(const bf16x8*)(phiq + (size_t)((((r * 32 + head) * 4 + wr) * 2 + mt) * 64 + lane) * 16);
  f32x4 s = {0, 0, 0, 0};
#pragma unroll
  for (int rr = 0; rr < 16; ++rr) s += *(const f32x4*)(kvp + (size_t)(((rr * 32 + head) * 4 + wr) * 64 + lane) * 4);
  *(f32x4*)(sm + w * 1024 + lane * 16) = s;
  __syncthreads();
  f32x4 t[4];
#pragma unroll
  for (int i = 0; i < 4; ++i) t[i] = *(const f32x4*)(sm + (kh * 4 + i) * 1024 + lane * 16);
  const bf16x8 kvA = cat8(pack4(t[0]), pack4(t[1])), zA = cat8(pack4(t[2]), pack4(t[3]));
  const f32x4 z4 = {0, 0, 0, 0};
#pragma unroll
  for (int mt = 0; mt < 2; ++mt) {
    f32x4 num = mfma32(kvA, pq[mt], z4), den = mfma32(zA, pq[mt], z4);
    const float inv = 1.f / (den[0] + 1e-6f);
    num *= inv;
    const int mtile = r * 8 + wr * 2 + mt;
    *(bf16x4*)(o + (size_t)(mtile * 16 + (head >> 1)) * 1024 + (m + 16 * (2 * (head & 1) + (gq >> 1))) * 16 + (gq & 1) * 8) = pack4(num);
  }
}

__global__ __launch_bounds__(512, 2) void k2_wo(const char* __restrict__ o, const unsigned short* __restrict__ wo, const unsigned short* __restrict__ x,
                                               const float* __restrict__ mod, char* __restrict__ x1, float* __restrict__ st2) {
  extern __shared__ __attribute__((aligned(16))) char sm[];
  const int tid = threadIdx.x, lane = tid & 63, w = __builtin_amdgcn_readfirstlane(tid >> 6);
  const int b = blockIdx.x, xcd = b & 7, idx = b >> 3;
  const int rb = xcd * 4 + (idx >> 3), cb = idx & 7;
  const int wm = w & 1, wn = (w >> 1) & 1, kh = w >> 2;
  const int m = lane & 15, gq = lane >> 4;
#pragma unroll
  for (int i = 0; i < 8; ++i) { const int id = w * 8 + i; glds16(o + (size_t)(rb * 64 + id) * 1024 + lane * 16, sm + id * 1024); }
#pragma unroll
  for (int i = 0; i < 8; ++i) {
    const int id = w * 8 + i, row = id * 8 + (lane >> 3);
    const int sw = ((row >> 1) & 1) | (((row >> 3) & 1) << 1);
    glds16(wo + (size_t)row * 512 + cb * 64 + (((lane & 7) ^ (sw << 1)) * 8), sm + 65536 + id * 1024);
  }
  WAITV(0);
  __syncthreads();
  f32x4 acc[2][2];
#pragma unroll
  for (int i = 0; i < 2; ++i)
#pragma unroll
    for (int j = 0; j < 2; ++j) acc[i][j] = f32x4{0, 0, 0, 0};
  {
    const int q = (lane & 15) >> 2, p = lane & 3;
    const int sw = (q >> 1) | ((gq & 1) << 1);
    const char* tb[2];
#pragma unroll
    for (int nt = 0; nt < 2; ++nt) tb[nt] = sm + 65536 + (kh * 256 + gq * 8 + q) * 128 + (((wn * 2 + nt) ^ sw) * 32) + p * 8;
#pragma unroll
    for (int ks = 0; ks < 8; ++ks) {
      bf16x8 af[2], wf[2];
#pragma unroll
      for (int mt = 0; mt < 2; ++mt) af[mt] = *(const bf16x8*)(sm + ((wm * 2 + mt) * 16 + kh * 8 + ks) * 1024 + lane * 16);
#pragma unroll
      for (int nt = 0; nt < 2; ++nt) wf[nt] = trfrag(tb[nt] + ks * 4096, 512);
#pragma unroll
      for (int mt = 0; mt < 2; ++mt)
#pragma unroll
        for (int nt = 0; nt < 2; ++nt) acc[mt][nt] = mfma32(wf[nt], af[mt], acc[mt][nt]);
    }
  }
  __syncthreads();
#pragma unroll
  for (int nt = 0; nt < 2; ++nt) *(f32x4*)(sm + (w * 2 + nt) * 1024 + lane * 16) = kh ? acc[0][nt] : acc[1][nt];
  __syncthreads();
  const int mtile = rb * 4 + wm * 2 + kh, row = mtile * 16 + m;
  float v[8];
#pragma unroll
  for (int nt = 0; nt < 2; ++nt) {
    const f32x4 a = (kh ? acc[1][nt] : acc[0][nt]) + *(const f32x4*)(sm + ((w ^ 4) * 2 + nt) * 1024 + lane * 16);
    const int n = cb * 64 + wn * 32 + nt * 16 + gq * 4;
    const u32x2 xv = *(const u32x2*)(x + (size_t)row * 512 + n);
    const f32x4 ga = *(const f32x4*)(mod + 1024 + n);
    v[nt * 4 + 0] = lo16(xv[0]) + ga[0] * a[0]; v[nt * 4 + 1] = hi16(xv[0]) + ga[1] * a[1];
    v[nt * 4 + 2] = lo16(xv[1]) + ga[2] * a[2]; v[nt * 4 + 3] = hi16(xv[1]) + ga[3] * a[3];
    u32x2 ov = {pack2(v[nt * 4], v[nt * 4 + 1]), pack2(v[nt * 4 + 2], v[nt * 4 + 3])};
    *(u32x2*)(x1 + (size_t)(mtile * 16 + cb * 2 + wn) * 1024 + (m + 16 * (2 * nt + (gq >> 1))) * 16 + (gq & 1) * 8) = ov;
  }
  float s = 0;
#pragma unroll
  for (int j = 0; j < 8; ++j) s += v[j];
  s = xsum32(xsum16(s));
  const float mean = s * (1.f / 32.f);
  float q2 = 0;
#pragma unroll
  for (int j = 0; j < 8; ++j) { float d = v[j] - mean; q2 += d * d; }
  q2 = xsum32(xsum16(q2));
  if (gq == 0) { f32x2 st = {mean, q2}; *(f32x2*)(st2 + (size_t)row * 32 + (cb * 2 + wn) * 2) = st; }
}

template <int NF>
DEVI void k3_body(char* sm, const char* __restrict__ x1, const unsigned short* __restrict__ wg, const unsigned short* __restrict__ wu,
                  const float* __restrict__ mod, const float* __restrict__ st2, char* __restrict__ hh, int r, int t0) {
  const int tid = threadIdx.x, lane = tid & 63, w = __builtin_amdgcn_readfirstlane(tid >> 6);
  const int wr = w & 3, kh = w >> 2;
  const int m = lane & 15, gq = lane >> 4;
  constexpr int STG = 49152;
  constexpr int MODOFF = 3 * STG;
  u32x4 xa[2][8];
#pragma unroll
  for (int mt = 0; mt < 2; ++mt)
#pragma unroll
    for (int ks = 0; ks < 8; ++ks) xa[mt][ks] = *(const u32x4*)(x1 + (size_t)((r * 8 + wr * 2 + mt) * 16 + kh * 8 + ks) * 1024 + lane * 16);
  { f32x2 mv = *(const f32x2*)(mod + 1536 + tid * 2); *(f32x2*)(sm + MODOFF + tid * 8) = mv; }
  float rs[2], nb[2];
#pragma unroll
  for (int mt = 0; mt < 2; ++mt) {
    const float* sp = st2 + (size_t)((r * 8 + wr * 2 + mt) * 16 + m) * 32 + gq * 8;
    const f32x4 a = *(const f32x4*)sp, bq = *(const f32x4*)(sp + 4);
    float sm_ = a[0] + a[2] + bq[0] + bq[2];
    sm_ = xsum32(xsum16(sm_));
    const float mean = sm_ * (1.f / 16.f);
    float d0 = a[0] - mean, d1 = a[2] - mean, d2 = bq[0] - mean, d3 = bq[2] - mean;
    float q = a[1] + a[3] + bq[1] + bq[3] + 32.f * (d0 * d0 + d1 * d1 + d2 * d2 + d3 * d3);
    q = xsum32(xsum16(q));
    rs[mt] = 1.f / sqrtf(q * (1.f / 512.f) + 1e-5f);
    nb[mt] = -mean * rs[mt];
  }
  auto stage = [&](int st) {
#pragma unroll
    for (int i = 0; i < 6; ++i) {
      const int id = w * 6 + i;
      const int khh = id / 24, mat = (id / 12) & 1, pair = (id >> 2) % 3, rbk = id & 3;
      const int rl = rbk * 16 + (lane >> 2);
      const int row = khh * 256 + st * 64 + rl;
      int sc = (lane & 3) ^ (((rl >> 3) & 1) << 1);
      int col = (t0 + pair * 2) * 16 + sc * 8;
      if (NF == 5 && pair == 2 && sc >= 2) col -= 16;
      const unsigned short* src = (mat ? wu : wg) + (size_t)row * 1376 + col;
      glds16(src, sm + (st % 3) * STG + id * 1024);
    }
  };
  stage(0); stage(1); stage(2);
  __syncthreads();
  bf16x8 ha[2][8];
#pragma unroll
  for (int ks = 0; ks < 8; ++ks) {
    const int k0 = kh * 256 + ks * 32 + gq * 8;
    const f32x4 sa0 = *(const f32x4*)(sm + MODOFF + k0 * 4), sa1 = *(const f32x4*)(sm + MODOFF + k0 * 4 + 16);
    const f32x4 s0 = *(const f32x4*)(sm + MODOFF + 2048 + k0 * 4), s1 = *(const f32x4*)(sm + MODOFF + 2048 + k0 * 4 + 16);
#pragma unroll
    for (int mt = 0; mt < 2; ++mt) {
      u32x4 o; const float a = rs[mt], c = nb[mt];
      o[0] = pack2((lo16(xa[mt][ks][0]) * a + c) * s0[0] + sa0[0], (hi16(xa[mt][ks][0]) * a + c) * s0[1] + sa0[1]);
      o[1] = pack2((lo16(xa[mt][ks][1]) * a + c) * s0[2] + sa0[2], (hi16(xa[mt][ks][1]) * a + c) * s0[3] + sa0[3]);
      o[2] = pack2((lo16(xa[mt][ks][2]) * a + c) * s1[0] + sa1[0], (hi16(xa[mt][ks][2]) * a + c) * s1[1] + sa1[1]);
      o[3] = pack2((lo16(xa[mt][ks][3]) * a + c) * s1[2] + sa1[2], (hi16(xa[mt][ks][3]) * a + c) * s1[3] + sa1[3]);
      ha[mt][ks] = __builtin_bit_cast(bf16x8, o);
    }
  }
  f32x4 acc[2][NF][2];
#pragma unroll
  for (int a = 0; a < 2; ++a)
#pragma unroll
    for (int f = 0; f < NF; ++f)
#pragma unroll
      for (int mt = 0; mt < 2; ++mt) acc[a][f][mt] = f32x4{0, 0, 0, 0};
  const int q = (lane & 15) >> 2, p = lane & 3;
  int tb[2];
#pragma unroll
  for (int hb = 0; hb < 2; ++hb) tb[hb] = kh * 24576 + (gq * 8 + q) * 64 + (((hb * 2 + (p >> 1)) ^ ((gq & 1) << 1)) * 16) + (p & 1) * 8;
#pragma unroll
  for (int st = 0; st < 4; ++st) {
    if (st == 0) { WAITV(12); } else if (st == 1) { WAITV(12); } else if (st == 2) { WAITV(6); } else { WAITV(0); }
    BAR();
    if (st == 1) stage(3);
    const char* sb = sm + (st % 3) * STG;
#pragma unroll
    for (int k2 = 0; k2 < 2; ++k2) {
      const int ks = st * 2 + k2;
#pragma unroll
      for (int a = 0; a < 2; ++a)
#pragma unroll
        for (int f = 0; f < NF; ++f) {
          const bf16x8 wf = trfrag(sb + tb[f & 1] + a * 12288 + (f >> 1) * 4096 + k2 * 2048, 256);
#pragma unroll
          for (int mt = 0; mt < 2; ++mt) acc[a][f][mt] = mfma32(wf, ha[mt][ks], acc[a][f][mt]);
        }
    }
  }
  __syncthreads();
#pragma unroll
  for (int a = 0; a < 2; ++a)
#pragma unroll
    for (int f = 0; f < NF; ++f) *(f32x4*)(sm + (w * 12 + a * 6 + f) * 1024 + lane * 16) = kh ? acc[a][f][0] : acc[a][f][1];
  __syncthreads();
  const int mtile = r * 8 + wr * 2 + kh;
#pragma unroll
  for (int f = 0; f < NF; ++f) {
    const f32x4 gv = (kh ? acc[0][f][1] : acc[0][f][0]) + *(const f32x4*)(sm + ((w ^ 4) * 12 + f) * 1024 + lane * 16);
    const f32x4 uv = (kh ? acc[1][f][1] : acc[1][f][0]) + *(const f32x4*)(sm + ((w ^ 4) * 12 + 6 + f) * 1024 + lane * 16);
    f32x4 hv;
#pragma unroll
    for (int j = 0; j < 4; ++j) {
      const float xg = gv[j];
      const float y2 = xg * (1.5957691216057308f + 0.07135481282670313f * xg * xg);
      hv[j] = xg / (1.f + __expf(-y2)) * uv[j];
    }
    const int ft = t0 + f;
    *(bf16x4*)(hh + (size_t)(mtile * 43 + (ft >> 1)) * 1024 + (m + 16 * (2 * (ft & 1) + (gq >> 1))) * 16 + (gq & 1) * 8) = pack4(hv);
  }
}
__global__ __launch_bounds__(512, 2) void k3_ffn1(const char* __restrict__ x1, const unsigned short* __restrict__ wg, const unsigned short* __restrict__ wu,
                                                 const float* __restrict__ mod, const float* __restrict__ st2, char* __restrict__ hh) {
  extern __shared__ __attribute__((aligned(16))) char sm[];
  const int b = blockIdx.x, xcd = b & 7, idx = b >> 3;
  const int r = (xcd >> 1) * 4 + (idx >> 3), cg = (xcd & 1) * 8 + (idx & 7);
  if (cg < 6) k3_body<6>(sm, x1, wg, wu, mod, st2, hh, r, cg * 6);
  else k3_body<5>(sm, x1, wg, wu, mod, st2, hh, r, 36 + (cg - 6) * 5);
}

__global__ __launch_bounds__(512, 2) void k4_ffn2(const char* __restrict__ hh, const unsigned short* __restrict__ wd, const char* __restrict__ x1,
                                                 const float* __restrict__ mod, unsigned short* __restrict__ out) {
  extern __shared__ __attribute__((aligned(16))) char sm[];
  const int tid = threadIdx.x, lane = tid & 63, w = __builtin_amdgcn_readfirstlane(tid >> 6);
  const int b = blockIdx.x, xcd = b & 7, idx = b >> 3;
  const int rb = xcd * 4 + (idx >> 3), cb = idx & 7;
  const int wm = w & 1, wn = (w >> 1) & 1, kh = w >> 2;
  const int m = lane & 15, gq = lane >> 4;
  auto stage = [&](int st) {
    const int khh = w >> 2, k2 = (w >> 1) & 1, part = w & 1;
    int ks = khh * 22 + st * 2 + k2; if (ks > 42) ks = 42;
    char* dst = sm + (st & 3) * 32768 + (khh * 2 + k2) * 8192 + part * 4096;
#pragma unroll
    for (int i = 0; i < 4; ++i) {
      if (part == 0) glds16(hh + (size_t)((rb * 4 + i) * 43 + ks) * 1024 + lane * 16, dst + i * 1024);
      else {
        const int rl = i * 8 + (lane >> 3);
        const int sw = ((rl >> 1) & 1) | (((rl >> 3) & 1) << 1);
        glds16(wd + (size_t)(ks * 32 + rl) * 512 + cb * 64 + (((lane & 7) ^ (sw << 1)) * 8), dst + i * 1024);
      }
    }
  };
  stage(0); stage(1); stage(2);
  f32x4 acc[2][2];
#pragma unroll
  for (int i = 0; i < 2; ++i)
#pragma unroll
    for (int j = 0; j < 2; ++j) acc[i][j] = f32x4{0, 0, 0, 0};
  const int q = (lane & 15) >> 2, p = lane & 3;
  const int sw = (q >> 1) | ((gq & 1) << 1);
  int tb[2];
#pragma unroll
  for (int nt = 0; nt < 2; ++nt) tb[nt] = kh * 16384 + 4096 + (gq * 8 + q) * 128 + (((wn * 2 + nt) ^ sw) * 32) + p * 8;
  const int ab = kh * 16384 + wm * 2048 + lane * 16;
  for (int st = 0; st < 11; ++st) {
    if (st < 9) { WAITV(8); } else if (st == 9) { WAITV(4); } else { WAITV(0); }
    BAR();
    if (st + 3 < 11) stage(st + 3);
    const char* sb = sm + (st & 3) * 32768;
#pragma unroll
    for (int k2 = 0; k2 < 2; ++k2) {
      if (k2 == 1 && st == 10 && kh == 1) break;
      bf16x8 af[2], wf[2];
#pragma unroll
      for (int mt = 0; mt < 2; ++mt) af[mt] = *(const bf16x8*)(sb + ab + k2 * 8192 + mt * 1024);
#pragma unroll
      for (int nt = 0; nt < 2; ++nt) wf[nt] = trfrag(sb + tb[nt] + k2 * 8192, 512);
#pragma unroll
      for (int mt = 0; mt < 2; ++mt)
#pragma unroll
        for (int nt = 0; nt < 2; ++nt) acc[mt][nt] = mfma32(wf[nt], af[mt], acc[mt][nt]);
    }
  }
  __syncthreads();
#pragma unroll
  for (int nt = 0; nt < 2; ++nt) *(f32x4*)(sm + (w * 2 + nt) * 1024 + lane * 16) = kh ? acc[0][nt] : acc[1][nt];
  __syncthreads();
  const int mtile = rb * 4 + wm * 2 + kh, row = mtile * 16 + m;
#pragma unroll
  for (int nt = 0; nt < 2; ++nt) {
    const f32x4 a = (kh ? acc[1][nt] : acc[0][nt]) + *(const f32x4*)(sm + ((w ^ 4) * 2 + nt) * 1024 + lane * 16);
    const int n = cb * 64 + wn * 32 + nt * 16 + gq * 4;
    const u32x2 xv = *(const u32x2*)(x1 + (size_t)(mtile * 16 + cb * 2 + wn) * 1024 + (m + 16 * (2 * nt + (gq >> 1))) * 16 + (gq & 1) * 8);
    const f32x4 gb = *(const f32x4*)(mod + 2560 + n);
    u32x2 ov = {pack2(lo16(xv[0]) + gb[0] * a[0], hi16(xv[0]) + gb[1] * a[1]), pack2(lo16(xv[1]) + gb[2] * a[2], hi16(xv[1]) + gb[3] * a[3])};
    *(u32x2*)(out + (size_t)row * 512 + n) = ov;
  }
}

extern "C" void kernel_launch(void* const* d_in, const int* in_sizes, int n_in, void* d_out, int out_size, void* d_ws, size_t ws_size,
                              hipStream_t stream) {
  const unsigned short* x = (const unsigned short*)d_in[0];
  const unsigned short* wqkv = (const unsigned short*)d_in[1];
  const unsigned short* wphi = (const unsigned short*)d_in[2];
  const unsigned short* wo = (const unsigned short*)d_in[3];
  const unsigned short* wg = (const unsigned short*)d_in[5];
  const unsigned short* wu = (const unsigned short*)d_in[6];
  const unsigned short* wd = (const unsigned short*)d_in[7];
  const unsigned short* c = (const unsigned short*)d_in[8];
  const unsigned short* wm = (const unsigned short*)d_in[9];
  char* ws = (char*)d_ws;
  float* mod = (float*)(ws + WS_MOD);
  static bool attr = false;
  if (!attr) {
    (void)hipFuncSetAttribute((const void*)k1_qkv, hipFuncAttributeMaxDynamicSharedMemorySize, 102400);
    (void)hipFuncSetAttribute((const void*)k2_wo, hipFuncAttributeMaxDynamicSharedMemorySize, 131072);
    (void)hipFuncSetAttribute((const void*)k3_ffn1, hipFuncAttributeMaxDynamicSharedMemorySize, 3 * 49152 + 4096);
    (void)hipFuncSetAttribute((const void*)k4_ffn2, hipFuncAttributeMaxDynamicSharedMemorySize, 131072);
    attr = true;
  }
  k0_mod_ln<<<192 + 128, 64, 0, stream>>>(x, c, wm, mod, ws + WS_XN);
  k1_qkv<<<256, 512, 102400, stream>>>(ws + WS_XN, wqkv, wphi, mod, ws + WS_PHIQ, (float*)(ws + WS_KVP));
  k1b_o<<<256, 512, 0, stream>>>(ws + WS_PHIQ, (float*)(ws + WS_KVP), ws + WS_O);
  k2_wo<<<256, 512, 131072, stream>>>(ws + WS_O, wo, x, mod, ws + WS_X1, (float*)(ws + WS_ST2));
  k3_ffn1<<<256, 512, 3 * 49152 + 4096, stream>>>(ws + WS_X1, wg, wu, mod, (float*)(ws + WS_ST2), ws + WS_HH);
  k4_ffn2<<<256, 512, 131072, stream>>>(ws + WS_HH, wd, ws + WS_X1, mod, (unsigned short*)d_out);
}
```

```cpp
#include <hip/hip_runtime.h>
#include <hip/hip_bf16.h>
#include <stdint.h>

typedef __attribute__((ext_vector_type(8))) short bf16x8;
typedef __attribute__((ext_vector_type(4))) short bf16x4;
typedef __attribute__((ext_vector_type(4))) float f32x4;
typedef __attribute__((ext_vector_type(2))) float f32x2;
typedef __attribute__((ext_vector_type(2))) __bf16 bf2_t;
typedef __attribute__((ext_vector_type(4))) unsigned u32x4;
typedef __attribute__((ext_vector_type(2))) unsigned u32x2;
#define LAS __attribute__((address_space(3)))
#define U2F(x) __builtin_bit_cast(float, (unsigned)(x))
#define F2U(x) __builtin_bit_cast(unsigned, (float)(x))
#if defined(__HIP_DEVICE_COMPILE__) && __HIP_DEVICE_COMPILE__
#define NOPK __attribute__((target("no-packed-fp32-ops")))
#else
#define NOPK
#endif
#define DEVI __device__ __forceinline__ NOPK

DEVI unsigned pack2(float a, float b) { f32x2 p = {a, b}; bf2_t q = __builtin_convertvector(p, bf2_t); return __builtin_bit_cast(unsigned, q); }
DEVI float lo16(unsigned u) { return U2F(u << 16); }
DEVI float hi16(unsigned u) { return U2F(u & 0xffff0000u); }
DEVI float vmax(float a, float b) { return __builtin_amdgcn_fmed3f(a, b, __builtin_inff()); }
DEVI float vmin(float a, float b) { return __builtin_amdgcn_fmed3f(a, b, -__builtin_inff()); }
DEVI float vmax3(float a, float b, float c) { return vmax(vmax(a, b), c); }
DEVI float vmin3(float a, float b, float c) { return vmin(vmin(a, b), c); }
DEVI float xmax16(float v) { auto r = __builtin_amdgcn_permlane16_swap(F2U(v), F2U(v), false, false); return vmax(U2F(r[0]), U2F(r[1])); }
DEVI float xmax32(float v) { auto r = __builtin_amdgcn_permlane32_swap(F2U(v), F2U(v), false, false); return vmax(U2F(r[0]), U2F(r[1])); }
DEVI float xmin16(float v) { auto r = __builtin_amdgcn_permlane16_swap(F2U(v), F2U(v), false, false); return vmin(U2F(r[0]), U2F(r[1])); }
DEVI float xmin32(float v) { auto r = __builtin_amdgcn_permlane32_swap(F2U(v), F2U(v), false, false); return vmin(U2F(r[0]), U2F(r[1])); }
DEVI float xsum16(float v) { auto r = __builtin_amdgcn_permlane16_swap(F2U(v), F2U(v), false, false); return U2F(r[0]) + U2F(r[1]); }
DEVI float xsum32(float v) { auto r = __builtin_amdgcn_permlane32_swap(F2U(v), F2U(v), false, false); return U2F(r[0]) + U2F(r[1]); }
template <int CTRL> DEVI float dpp(float v) { return U2F(__builtin_amdgcn_update_dpp(0u, F2U(v), CTRL, 0xf, 0xf, true)); }
DEVI float xsum1(float v) { return v + dpp<0xB1>(v); }
DEVI float xsum2(float v) { return v + dpp<0x4E>(v); }
DEVI float fexp2(float x) { return __builtin_amdgcn_exp2f(x); }
DEVI float frcp(float x) { return __builtin_amdgcn_rcpf(x); }
DEVI f32x4 mfma32(bf16x8 a, bf16x8 b, f32x4 c) { return __builtin_amdgcn_mfma_f32_16x16x32_bf16(a, b, c, 0, 0, 0); }
DEVI f32x4 mfma16(bf16x4 a, bf16x4 b, f32x4 c) { return __builtin_amdgcn_mfma_f32_16x16x16bf16_1k(a, b, c, 0, 0, 0); }
DEVI bf16x4 pack4(f32x4 v) { u32x2 u = {pack2(v[0], v[1]), pack2(v[2], v[3])}; return __builtin_bit_cast(bf16x4, u); }
DEVI bf16x8 cat8(bf16x4 a, bf16x4 b) { return __builtin_shufflevector(a, b, 0, 1, 2, 3, 4, 5, 6, 7); }
DEVI void glds16(const void* g, unsigned l) {
  unsigned keep;
  asm volatile("s_mov_b32 %0, m0\n\ts_mov_b32 m0, %2\n\ts_nop 0\n\tglobal_load_lds_dwordx4 %1, off\n\ts_mov_b32 m0, %0" : "=&s"(keep) : "v"(g), "s"(l) : "memory");
}
DEVI bf16x4 trread(const char* lds) { return __builtin_amdgcn_ds_read_tr16_b64_v4i16((LAS bf16x4*)lds); }
DEVI bf16x8 trfrag(const char* lds, int step) { return cat8(trread(lds), trread(lds + step)); }
#define WAITV(n) asm volatile("s_waitcnt vmcnt(" #n ")" ::: "memory")
#define BAR() do { asm volatile("s_waitcnt lgkmcnt(0)" ::: "memory"); __builtin_amdgcn_s_barrier(); asm volatile("" ::: "memory"); } while (0)
#define STAMP_DECL
#define TS(i)
#define STAMP_FLUSH(K, wsbase)
#define TSARG
#define TSPASS
#define FENCE() __builtin_amdgcn_sched_barrier(0)
template <int I> struct IC { static constexpr int value = I; };
template <int I, int N, class F> DEVI void static_for(F&& f) { if constexpr (I < N) { f(IC<I>{}); static_for<I + 1, N>(f); } }
typedef unsigned long long u64;
constexpr size_t WS_FLG = 0;
constexpr int F_E = 0, F_S1 = 16, F_S2 = 16 * 17, F_S3 = 16 * 33, F_S4 = 16 * 49, F_S5 = 16 * 65, F_RT = 16 * 81;
constexpr size_t WS_MODP = 64 << 10;
constexpr size_t WS_H = 1 << 20;
constexpr size_t WS_KVP = 4 << 20;
constexpr size_t WS_O = 8 << 20;
constexpr size_t WS_X1 = 12 << 20;
constexpr size_t WS_ST2 = 16 << 20;
constexpr size_t WS_HH = 20 << 20;
constexpr size_t WS_WT = 32 << 20;
constexpr unsigned WT_UP = 0;
constexpr unsigned WT_W3 = 1 << 20;
constexpr unsigned WT_W4 = 4 << 20;
constexpr int KVP_STRIDE = 2304;

#define GLD16(dst, sbase, voff, imm) asm volatile("global_load_dwordx4 %0, %1, %2 offset:%3" : "=v"(dst) : "v"(voff), "s"(sbase), "i"(imm) : "memory")
#define GLD16S(dst, sbase, voff, imm) asm volatile("global_load_dwordx4 %0, %1, %2 offset:%3 sc1" : "=v"(dst) : "v"(voff), "s"(sbase), "i"(imm) : "memory")
#define GLD8(dst, sbase, voff, imm) asm volatile("global_load_dwordx2 %0, %1, %2 offset:%3" : "=v"(dst) : "v"(voff), "s"(sbase), "i"(imm) : "memory")
#define GLD8S(dst, sbase, voff, imm) asm volatile("global_load_dwordx2 %0, %1, %2 offset:%3 sc1" : "=v"(dst) : "v"(voff), "s"(sbase), "i"(imm) : "memory")
#define GLDU16(dst, sbase, voff, imm) asm volatile("global_load_ushort %0, %1, %2 offset:%3" : "=v"(dst) : "v"(voff), "s"(sbase), "i"(imm) : "memory")
#define WV1(n, a) asm volatile("s_waitcnt vmcnt(%1)" : "+v"(a) : "i"(n) : "memory")
#define WV2(n, a, b) asm volatile("s_waitcnt vmcnt(%2)" : "+v"(a), "+v"(b) : "i"(n) : "memory")
#define WV4(n, a, b, c, d) asm volatile("s_waitcnt vmcnt(%4)" : "+v"(a), "+v"(b), "+v"(c), "+v"(d) : "i"(n) : "memory")
#define WVN(n) asm volatile("s_waitcnt vmcnt(%0)" :: "i"(n) : "memory")
DEVI void gblk(const void* sbase, unsigned voff, unsigned l) {
  unsigned keep;
  asm volatile("s_mov_b32 %0, m0\n\ts_mov_b32 m0, %3\n\ts_nop 0\n\tglobal_load_lds_dwordx4 %1, %2\n\ts_mov_b32 m0, %0" : "=&s"(keep) : "v"(voff), "s"(sbase), "s"(l) : "memory");
}
DEVI void gblks(const void* sbase, unsigned voff, unsigned l) {
  unsigned keep;
  asm volatile("s_mov_b32 %0, m0\n\ts_mov_b32 m0, %3\n\ts_nop 0\n\tglobal_load_lds_dwordx4 %1, %2 sc1\n\ts_mov_b32 m0, %0" : "=&s"(keep) : "v"(voff), "s"(sbase), "s"(l) : "memory");
}

DEVI void featmap(f32x4 p, bf16x4& pos, bf16x4& neg) {
  constexpr float L2E = 1.4426950408889634f;
  float mx = vmax(vmax(p[0], p[1]), vmax(p[2], p[3]));
  float mn = vmin(vmin(p[0], p[1]), vmin(p[2], p[3]));
  mx = xmax32(xmax16(mx)); mn = xmin32(xmin16(mn));
  f32x4 ep, en;
#pragma unroll
  for (int j = 0; j < 4; ++j) { ep[j] = fexp2((p[j] - mx) * L2E); en[j] = fexp2((mn - p[j]) * L2E); }
  float sp = (ep[0] + ep[1]) + (ep[2] + ep[3]), sn = (en[0] + en[1]) + (en[2] + en[3]);
  sp = xsum32(xsum16(sp)); sn = xsum32(xsum16(sn));
  const float ip = frcp(sp), in = frcp(sn);
  ep *= ip; en *= in;
  pos = pack4(ep); neg = pack4(en);
}

typedef __amdgpu_buffer_rsrc_t rsrc_t;
DEVI rsrc_t mkrsrc(const void* p, unsigned bytes) { return __builtin_amdgcn_make_buffer_rsrc((void*)p, 0, bytes, 0x00020000); }
DEVI void st16_sc1(rsrc_t r, unsigned off, u32x4 v) { __builtin_amdgcn_raw_buffer_store_b128(v, r, off, 0, 16); }
DEVI void st8_sc1(rsrc_t r, unsigned off, u32x2 v) { __builtin_amdgcn_raw_buffer_store_b64(v, r, off, 0, 16); }
DEVI void st4_sc1(rsrc_t r, unsigned off, unsigned v) { __builtin_amdgcn_raw_buffer_store_b32(v, r, off, 0, 16); }
DEVI u64 ldflag(const u64* p) { return __hip_atomic_load(p, __ATOMIC_RELAXED, __HIP_MEMORY_SCOPE_AGENT); }
DEVI void stflag(u64* p, u64 v) { __hip_atomic_store(p, v, __ATOMIC_RELAXED, __HIP_MEMORY_SCOPE_AGENT); }

DEVI void signal(u64* f, u64 tag) {
  WAITV(0);
  BAR();
  if (threadIdx.x == 0) stflag(f, tag);
}
DEVI void spinflags(const u64* f, int n, u64 tag, int lane) {
  const u64* p = f + (lane < n ? lane : 0);
  unsigned spins = 0;
  while (__builtin_amdgcn_ballot_w64(ldflag(p) != tag) != 0ull) {
    __builtin_amdgcn_s_sleep(1);
    if (++spins > (1u << 20)) break;
  }
}
DEVI void waitflags(const u64* f, int n, u64 tag, int w, int lane) {
  if (w == 0) spinflags(f, n, tag, lane);
  BAR();
}

constexpr int SLOT = 24576, L_TOP = 147456;
constexpr int L_W2 = 98304;
constexpr int L_SCR = 147456, L_MOD1 = 149504, L_HT = 153600;
constexpr int L_M2V = 49152, L_GV = 53248;
DEVI constexpr int w1_off(int c) { return c * SLOT; }
DEVI constexpr int w3_slot(int ks) { return (ks % 6) * SLOT; }
DEVI constexpr int w4_off(int i) { return i < 12 ? 2 * SLOT + i * 8192 : i < 18 ? (i - 12) * 8192 : i < 20 ? L_TOP + (i - 18) * 8192 : 2 * SLOT + (i - 20) * 8192; }
DEVI void w3_piece(const char* wt, unsigned l16, int t0, bool nf5, int ks, int id) {
  const int khh = id / 12, mat = (id / 6) & 1; int f = id % 6;
  const int fs = (nf5 && f == 5) ? 4 : f;
  gblks(wt + WT_W3 + (size_t)((mat * 86 + t0 + fs) * 16 + khh * 8 + ks) * 1024, l16, w3_slot(ks) + id * 1024);
}
DEVI void w4_piece(const char* wt, unsigned l16, int cb, int i, int pc) {
  const int khh = pc >> 2, nt = pc & 3;
  int ks = khh * 22 + i; if (ks > 42) ks = 42;
  gblks(wt + WT_W4 + (size_t)((cb * 4 + nt) * 43 + ks) * 1024, l16, w4_off(i) + pc * 1024);
}

DEVI float sfma(float a, float b, float c) { return __builtin_fmaf(a, b, c); }
DEVI bf16x8 modulate2(u32x4 xv, float a, float c, f32x4 s0, f32x4 s1, f32x4 a0, f32x4 a1) {
  u32x4 o;
  o[0] = pack2(sfma(sfma(lo16(xv[0]), a, c), s0[0], a0[0]), sfma(sfma(hi16(xv[0]), a, c), s0[1], a0[1]));
  o[1] = pack2(sfma(sfma(lo16(xv[1]), a, c), s0[2], a0[2]), sfma(sfma(hi16(xv[1]), a, c), s0[3], a0[3]));
  o[2] = pack2(sfma(sfma(lo16(xv[2]), a, c), s1[0], a1[0]), sfma(sfma(hi16(xv[2]), a, c), s1[1], a1[1]));
  o[3] = pack2(sfma(sfma(lo16(xv[3]), a, c), s1[2], a1[2]), sfma(sfma(hi16(xv[3]), a, c), s1[3], a1[3]));
  return __builtin_bit_cast(bf16x8, o);
}
DEVI float dot2(unsigned a, unsigned b, float c) { return __builtin_amdgcn_fdot2_f32_bf16(__builtin_bit_cast(bf2_t, a), __builtin_bit_cast(bf2_t, b), c, false); }
#define LDF(p) (*(const bf16x8*)(p))
#define LDV4(p) (*(const f32x4*)(p))
struct LB { const LAS char* b[3]; };
DEVI LB mklb(unsigned off) { LB l; unsigned a0 = off, a1 = off + 65536, a2 = off + 131072; asm("" : "+v"(a0)); asm("" : "+v"(a1)); asm("" : "+v"(a2));
  l.b[0] = (const LAS char*)(size_t)a0; l.b[1] = (const LAS char*)(size_t)a1; l.b[2] = (const LAS char*)(size_t)a2; return l; }
#define LDB(lb, off) (*(const LAS bf16x8*)((lb).b[(off) >> 16] + ((off) & 65535)))
#define TRB(lb, off) __builtin_amdgcn_ds_read_tr16_b64_v4i16((LAS bf16x4*)((lb).b[(off) >> 16] + ((off) & 65535)))
#define MIX(n, k) _Pragma("unroll") for (int i_ = 0; i_ < (n); ++i_) { __builtin_amdgcn_sched_group_barrier(0x008, 1, 0); __builtin_amdgcn_sched_group_barrier(0x002, (k), 0); }

DEVI void gemv_part(const unsigned c2, const u32x4 (&wm)[2], char* scr, int w, int lane) {
  float acc[8];
  const float c0 = lo16(c2), c1 = hi16(c2);
  const float s0 = c0 * frcp(1.f + fexp2(c0 * -1.4426950408889634f)), s1 = c1 * frcp(1.f + fexp2(c1 * -1.4426950408889634f));
#pragma unroll
  for (int j = 0; j < 4; ++j) { acc[2 * j] = sfma(s1, lo16(wm[1][j]), s0 * lo16(wm[0][j])); acc[2 * j + 1] = sfma(s1, hi16(wm[1][j]), s0 * hi16(wm[0][j])); }
#pragma unroll
  for (int j = 0; j < 8; ++j) { float v = acc[j]; v += dpp<0x128>(v); acc[j] = xsum32(xsum16(v)); }
  if (lane < 8) {
    float* d = (float*)scr + w * 64 + lane * 8;
    *(f32x4*)d = f32x4{acc[0], acc[1], acc[2], acc[3]}; *(f32x4*)(d + 4) = f32x4{acc[4], acc[5], acc[6], acc[7]};
  }
}
DEVI f32x2 modsum(const u32x4 (&g)[4]) {
  return f32x2{(U2F(g[0][0]) + U2F(g[1][0])) + (U2F(g[2][0]) + U2F(g[3][0])), (U2F(g[0][2]) + U2F(g[1][2])) + (U2F(g[2][2]) + U2F(g[3][2]))};
}
#define GLD4(dst, sbase, voff, imm) asm volatile("global_load_dword %0, %1, %2 offset:%3" : "=v"(dst) : "v"(voff), "s"(sbase), "i"(imm) : "memory")
#define MODLD(g, voff) { GLD16S(g[0], modp, voff, 0); GLD16S(g[1], modp + 24576, voff, 0); GLD16S(g[2], modp + 49152, voff, 0); GLD16S(g[3], modp + 73728, voff, 0); }

template <int NF>
DEVI void phase3(char* sm, char* ws, int r, int t0, int cb, int w, int lane, u64 tag TSARG) {
  u64* flg = (u64*)(ws + WS_FLG);
  const int wr = w & 3, kh = w >> 2, m = lane & 15, gq = lane >> 4;
  const unsigned l16 = lane * 16;
  const char* wt = ws + WS_WT;
  const rsrc_t rhh = mkrsrc(ws + WS_HH, 128 * 43 * 1024);
  const int mtile = r * 8 + wr * 2 + kh;
  u32x4 xa[2][8], sta[2];
  {
    const char* xb0 = ws + WS_X1 + (size_t)((r * 8 + wr * 2) * 16 + kh * 8) * 1024;
#pragma unroll
    for (int ks = 0; ks < 8; ++ks) {
      const char* xb = xb0 + (ks >> 2) * 4096;
      GLD16S(xa[0][ks], xb, l16, (ks & 3) * 1024);
      GLD16S(xa[1][ks], xb + 16384, l16, (ks & 3) * 1024);
    }
  }
  const char* stb = ws + WS_ST2 + (size_t)(mtile * 16) * 128;
  u32x4& sta0 = sta[0]; u32x4& sta1 = sta[1];
  const LB fb = mklb(kh * 12288 + l16);
  bf16x8 wf[3][3];
#define P3_LD(gi_) _Pragma("unroll") for (int f3 = 0; f3 < 3; ++f3) { const int f = ((gi_) & 1) * 3 + f3; if (f < NF) wf[(gi_) % 3][f3] = LDB(fb, w3_slot((gi_) >> 2) + (((gi_) >> 1) & 1) * 6144 + f * 1024); }
#define P3_MM(gi_) _Pragma("unroll") for (int f3 = 0; f3 < 3; ++f3) { const int f = ((gi_) & 1) * 3 + f3, a_ = ((gi_) >> 1) & 1, ks_ = (gi_) >> 2; if (f < NF) { acc[a_][f][0] = mfma32(wf[(gi_) % 3][f3], __builtin_bit_cast(bf16x8, xa[0][ks_]), acc[a_][f][0]); acc[a_][f][1] = mfma32(wf[(gi_) % 3][f3], __builtin_bit_cast(bf16x8, xa[1][ks_]), acc[a_][f][1]); } }
  P3_LD(0); P3_LD(1);
  f32x4 acc[2][NF][2];
#pragma unroll
  for (int a = 0; a < 2; ++a)
#pragma unroll
    for (int f = 0; f < NF; ++f)
#pragma unroll
      for (int mt = 0; mt < 2; ++mt) acc[a][f][mt] = f32x4{0, 0, 0, 0};
  TS(19);
  static_for<0, 32>([&](auto GI) __attribute__((always_inline)) NOPK {
    constexpr int gi = decltype(GI)::value;
    constexpr int ks = gi >> 2, ph = gi & 3;
    constexpr int pl = (gi > 6 ? (gi < 12 ? ((gi - 6) >> 1) * 2 : 6) : 0) + (gi > 14 ? (gi < 20 ? ((gi - 14) >> 1) * 2 : 6) : 0) + (gi > 22 ? (gi < 28 ? ((gi - 22) >> 1) * 2 : 6) : 0);
    if constexpr (gi == 6) BAR();
    if constexpr (gi == 14) BAR();
    if constexpr (gi == 22) { WVN(8); BAR(); }
    if constexpr (gi + 2 < 32) P3_LD(gi + 2);
    if constexpr (ph == 0) { WV2(2 * (7 - ks) + pl, xa[0][ks], xa[1][ks]); }
    if constexpr (gi == 21) { const unsigned so = (unsigned)(m * 128 + gq * 32); GLD16S(sta0, stb, so, 0); GLD16S(sta1, stb, so, 16); }
    FENCE();
    P3_MM(gi);
    FENCE();
    if constexpr (gi >= 6 && gi < 12) { if (kh == (gi & 1)) { constexpr int q = ((gi - 6) >> 1) * 2; w3_piece(wt, l16, t0, NF == 5, 6 + q / 3, w * 3 + q % 3); w3_piece(wt, l16, t0, NF == 5, 6 + (q + 1) / 3, w * 3 + (q + 1) % 3); } }
    if constexpr (gi >= 14 && gi < 20) { if (kh == (gi & 1)) { constexpr int q = ((gi - 14) >> 1) * 2; w4_piece(wt, l16, cb, q, w); w4_piece(wt, l16, cb, q + 1, w); } }
    if constexpr (gi >= 22 && gi < 28) { if (kh == (gi & 1)) { constexpr int q = 6 + ((gi - 22) >> 1) * 2; w4_piece(wt, l16, cb, q, w); w4_piece(wt, l16, cb, q + 1, w); } }
  });
  TS(20);
  float rs, nb;
  {
    WV2(6, sta[0], sta[1]);
    const f32x4 a = __builtin_bit_cast(f32x4, sta[0]), bq = __builtin_bit_cast(f32x4, sta[1]);
    float sm_ = (a[0] + a[2]) + (bq[0] + bq[2]);
    sm_ = xsum32(xsum16(sm_));
    const float mean = sm_ * (1.f / 16.f);
    float d0 = a[0] - mean, d1 = a[2] - mean, d2 = bq[0] - mean, d3 = bq[2] - mean;
    float q = (a[1] + a[3]) + (bq[1] + bq[3]) + 32.f * ((d0 * d0 + d1 * d1) + (d2 * d2 + d3 * d3));
    q = xsum32(xsum16(q));
    rs = 1.f / __builtin_sqrtf(q * (1.f / 512.f) + 1e-5f);
    nb = -mean * rs;
  }
  BAR();
  char* xw = sm + w * 6144 + l16;
  const char* xr = sm + (w ^ 4) * 6144 + l16;
  const char* ub = sm + L_TOP + gq * 16;
#pragma unroll
  for (int rd = 0; rd < 2; ++rd) {
    if (rd == 1) BAR();
#pragma unroll
    for (int a = 0; a < 2; ++a)
#pragma unroll
      for (int f3 = 0; f3 < 3; ++f3) { const int f = rd * 3 + f3; if (f < NF) *(f32x4*)(xw + (a * 3 + f3) * 1024) = kh ? acc[a][f][0] : acc[a][f][1]; }
    BAR();
#pragma unroll
    for (int f3 = 0; f3 < 3; ++f3) {
      const int f = rd * 3 + f3;
      if (f < NF) {
        const f32x4 gv = (kh ? acc[0][f][1] : acc[0][f][0]) + *(const f32x4*)(xr + f3 * 1024);
        const f32x4 uv = (kh ? acc[1][f][1] : acc[1][f][0]) + *(const f32x4*)(xr + (3 + f3) * 1024);
        const f32x4 g1 = LDV4(ub + f * 64), g2 = LDV4(ub + 768 + f * 64), u1 = LDV4(ub + 384 + f * 64), u2 = LDV4(ub + 1152 + f * 64);
        f32x4 hv;
#pragma unroll
        for (int j = 0; j < 4; ++j) {
          const float xg = sfma(rs, gv[j], sfma(nb, g1[j], g2[j]));
          const float xu = sfma(rs, uv[j], sfma(nb, u1[j], u2[j]));
          const float y2 = xg * (-2.302208198f - 0.1029432397f * xg * xg);
          hv[j] = xg * frcp(1.f + fexp2(y2)) * xu;
        }
        const int ft = t0 + f;
        st8_sc1(rhh, (unsigned)((mtile * 43 + (ft >> 1)) * 1024 + (m + 16 * (2 * (ft & 1) + (gq >> 1))) * 16 + (gq & 1) * 8), __builtin_bit_cast(u32x2, pack4(hv)));
      }
    }
  }
  TS(21);
  signal(flg + F_S5 + r * 16 + (blockIdx.x >> 3 & 15), tag);
  TS(22);
}

__global__ __launch_bounds__(512) __attribute__((amdgpu_waves_per_eu(2, 2))) NOPK
void k1_main(const unsigned short* __restrict__ x, const unsigned short* __restrict__ wqkv, const unsigned short* __restrict__ wphi,
             const unsigned short* __restrict__ wo, const unsigned short* __restrict__ wg, const unsigned short* __restrict__ wu,
             const unsigned short* __restrict__ wd, const unsigned short* __restrict__ cvec, const unsigned short* __restrict__ wmod,
             char* __restrict__ ws, unsigned short* __restrict__ out) {
  extern __shared__ __attribute__((aligned(16))) char sm[];
  STAMP_DECL TS(0);
  const int tid = threadIdx.x, lane = tid & 63, w = __builtin_amdgcn_readfirstlane(tid >> 6);
  const int b = blockIdx.x, xcd = b & 7, idx = b >> 3;
  const int r = xcd * 2 + (idx >> 4), j = idx & 15;
  const int m = lane & 15, gq = lane >> 4;
  const int kh = w >> 2, wr = w & 3;
  u64* flg = (u64*)(ws + WS_FLG);
  const f32x4 z4 = {0, 0, 0, 0};
  const int rh = j >> 3, cb = j & 7;
  const int t0 = j < 6 ? j * 6 : 36 + (j - 6) * 5;
  const bool nf5 = j >= 6;
  const unsigned l16 = lane * 16;
  const char* wt = ws + WS_WT;
  const char* modp = ws + WS_MODP;
  bf16x8 pq[2];
  bf16x8 w2f[8][4];
  f32x4 gav[2], gbv[2];
  const int n2 = cb * 64 + kh * 32 + gq * 4;
  u64 tag;
  {
    const int g = j, head = 2 * g + kh;
    u32x2 ev; unsigned c2; u32x4 wm[2], xr; unsigned wp0, wp1, wp2, wp3;
    const int bb = b < 192 ? b : b - 64, slab = bb >> 2, kq = bb & 3;
    const unsigned grow = (unsigned)(kq * 128 + w * 16 + (lane >> 3) * 2);
    GLD8S(ev, flg, 0u, 0);
    GLD4(c2, cvec, grow * 2, 0);
    {
      const char* wb = (const char*)(wmod + slab * 64);
      const unsigned wmo = grow * 6144 + (lane & 7) * 16;
      GLD16(wm[0], wb, wmo, 0); GLD16(wm[1], wb + 6144, wmo, 0);
    }
    GLD16(xr, (const char*)(x + (size_t)(r * 128 + j * 8 + w) * 512), l16, 0);
    {
      const char* wpb = (const char*)(wphi + head * 256);
      const unsigned o = (unsigned)(gq * 128 + m * 2);
      GLDU16(wp0, wpb, o, 0); GLDU16(wp1, wpb, o, 32); GLDU16(wp2, wpb, o, 64); GLDU16(wp3, wpb, o, 96);
    }
    __builtin_amdgcn_s_barrier();
    unsigned wu_;
    {
      const char* tb_ = w < 6 ? (const char*)wqkv + (idx * 6 + w) * 8192 : (const char*)wo + (idx * 2 + w - 6) * 8192;
      GLD4(wu_, tb_, (unsigned)(lane * 128), 0);
    }
    TS(27);
    WV4(1, c2, wm[0], wm[1], ev); WV4(1, wp0, wp1, wp2, wp3); WV1(1, xr);
    TS(28);
    tag = (((u64)__builtin_amdgcn_readfirstlane(ev[1]) << 32) | (u64)(unsigned)__builtin_amdgcn_readfirstlane(ev[0])) + 1ull;
    gemv_part(c2, wm, sm + L_SCR, w, lane);
    BAR();
    TS(1);
    if (w == 0 && b < 192) {
      float s = 0;
#pragma unroll
      for (int p = 0; p < 8; ++p) s += ((const float*)(sm + L_SCR))[p * 64 + lane];
      st8_sc1(mkrsrc(ws + WS_MODP, 98304), (unsigned)(kq * 24576 + (slab * 64 + lane) * 8), u32x2{F2U(s), (unsigned)tag});
    }
    const unsigned w1o = (unsigned)((lane >> 2) * 3072 + (((lane & 3) ^ (((lane >> 5) & 1) << 1)) * 16));
#define P1_W(c_, pc_) { const int pc__ = (pc_); const int khh_ = pc__ / 12, seg_ = (pc__ >> 2) % 3, rbk_ = pc__ & 3; \
      gblk((const char*)(wqkv + (size_t)(khh_ * 256 + (c_) * 64 + rbk_ * 16) * 1536 + seg_ * 512 + g * 32), w1o, w1_off(c_) + pc__ * 1024); }
#pragma unroll
    for (int i = 0; i < 6; ++i) P1_W(i / 3, w * 3 + i % 3);
    float xf[8], rs1, nb1;
    {
#pragma unroll
      for (int i = 0; i < 4; ++i) { xf[2 * i] = lo16(xr[i]); xf[2 * i + 1] = hi16(xr[i]); }
      float s = ((xf[0] + xf[1]) + (xf[2] + xf[3])) + ((xf[4] + xf[5]) + (xf[6] + xf[7]));
      s = xsum2(xsum1(s)); s += dpp<0x124>(s); s += dpp<0x128>(s); s = xsum32(xsum16(s));
      const float mean = s * (1.f / 512.f);
      float q = 0;
#pragma unroll
      for (int i = 0; i < 8; ++i) { const float d = xf[i] - mean; q = sfma(d, d, q); }
      q = xsum2(xsum1(q)); q += dpp<0x124>(q); q += dpp<0x128>(q); q = xsum32(xsum16(q));
      rs1 = 1.f / __builtin_sqrtf(q * (1.f / 512.f) + 1e-5f);
      nb1 = -mean * rs1;
    }
    TS(29);
    __builtin_amdgcn_s_barrier();
    {
      u32x4 mg[4];
      MODLD(mg, tid * 16);
      WV4(0, mg[0], mg[1], mg[2], mg[3]); WV1(0, wu_);
      const unsigned tl = (unsigned)tag;
      unsigned spins = 0;
      while (true) {
        bool ok = true;
#pragma unroll
        for (int k = 0; k < 4; ++k) ok = ok && mg[k][1] == tl && mg[k][3] == tl;
        if (__builtin_amdgcn_ballot_w64(!ok) == 0ull || ++spins > (1u << 18)) break;
        __builtin_amdgcn_s_sleep(1);
        MODLD(mg, tid * 16);
        WV4(0, mg[0], mg[1], mg[2], mg[3]);
      }
      TS(30);
      f32x2 mv = modsum(mg);
      if (tid >= 256) { mv[0] += 1.f; mv[1] += 1.f; }
      *(f32x2*)(sm + L_MOD1 + tid * 8) = mv;
      BAR();
      const char* a_ = sm + L_MOD1 + lane * 32;
      const f32x4 a0 = LDV4(a_), a1 = LDV4(a_ + 16), s0 = LDV4(a_ + 2048), s1 = LDV4(a_ + 2064);
      const bf16x8 hv = modulate2(xr, rs1, nb1, s0, s1, a0, a1);
      *(bf16x8*)(sm + L_HT + lane * 128 + ((w ^ (lane & 7)) * 16)) = hv;
      BAR();
      const int seg = tid >> 3, i8 = tid & 7;
      const u32x4 ho = *(const u32x4*)(sm + L_HT + seg * 128 + ((i8 ^ (seg & 7)) * 16));
      st16_sc1(mkrsrc(ws + WS_H, 2 << 20), (unsigned)(((r * 8 + (j >> 1)) * 16 + (seg >> 2)) * 1024 + ((j & 1) * 8 + i8 + 16 * (seg & 3)) * 16), ho);
    }
    TS(2);
    signal(flg + F_S1 + r * 16 + j, tag);
    TS(3);
    u32x4 tv[4]; int nvalid, rks; unsigned rdst, rstep, rup; bool risw3;
    const char* rsb; unsigned rso; int rpitch;
    {
      int u = w * 256 + b; const bool have = u < 1048; if (!have) u = b;
      const unsigned short* src; int nbk; nvalid = 4; risw3 = u < 704; rup = 0;
      if (risw3) { const int mat = u / 352, v2 = u % 352; rks = v2 / 22; nbk = v2 % 22; src = mat ? wu : wg; rpitch = 1376;
        rdst = WT_W3 + (unsigned)((mat * 86 + nbk * 4) * 16 + rks) * 1024; rstep = 16 * 1024; if (nbk == 21) nvalid = 2;
        rup = WT_UP + (unsigned)((mat * 16 + rks) * 1376 + nbk * 64) * 8; }
      else { const int v = u - 704; rks = v >> 3; nbk = v & 7; src = wd; rpitch = 512; rdst = WT_W4 + (unsigned)(nbk * 4 * 43 + rks) * 1024; rstep = 43 * 1024; }
      int ch = lane & 7; if (nvalid == 2) ch &= 3;
      if (!have) nvalid = 0;
      rsb = (const char*)(src + (size_t)(rks * 32) * rpitch + nbk * 64);
      rso = (unsigned)((lane >> 3) * rpitch * 2 + ch * 16);
    }
    if (w >= 2) {
#pragma unroll
      for (int i = 0; i < 8; ++i) { const int id = (w - 2) * 8 + i; P1_W(2 + id / 24, id % 24); }
    }
    waitflags(flg + F_S1 + r * 16, 16, tag, w, lane);
    TS(4);
    f32x4 acc[3][2][2];
#pragma unroll
    for (int s = 0; s < 3; ++s)
#pragma unroll
      for (int hb = 0; hb < 2; ++hb)
#pragma unroll
        for (int mt = 0; mt < 2; ++mt) acc[s][hb][mt] = z4;
    u32x4 m2[4], gv[4];
    const unsigned gvo = (unsigned)(((lane & 32) ? 2560 : 1024) + cb * 64 + (lane & 31) * 2) * 8;
    {
      u32x4 ha[2][8];
      const char* hb0 = ws + WS_H + (size_t)((r * 8 + wr * 2) * 16 + kh * 8) * 1024;
#pragma unroll
      for (int ks = 0; ks < 8; ++ks) {
        const char* hb_ = hb0 + (ks >> 2) * 4096;
        GLD16S(ha[0][ks], hb_, l16, (ks & 3) * 1024);
        GLD16S(ha[1][ks], hb_ + 16384, l16, (ks & 3) * 1024);
      }
      const int q = (lane & 15) >> 2, p = lane & 3;
      LB tb[2];
#pragma unroll
      for (int hb = 0; hb < 2; ++hb) tb[hb] = mklb(kh * 12288 + (gq * 8 + q) * 64 + (((hb * 2 + (p >> 1)) ^ ((gq & 1) << 1)) * 16) + (p & 1) * 8);
      bf16x8 wf[2][6];
#define P1_LD(s_) _Pragma("unroll") for (int i = 0; i < 6; ++i) { constexpr int o_ = w1_off((s_) >> 1) + ((s_) & 1) * 2048; wf[(s_) & 1][i] = cat8(TRB(tb[i & 1], o_ + (i >> 1) * 4096), TRB(tb[i & 1], o_ + (i >> 1) * 4096 + 256)); }
      P1_LD(0);
      FENCE();
      TS(5);
      const unsigned w2o = (unsigned)((lane >> 3) * 1024 + (((lane & 7) ^ ((((lane >> 4) & 1) | ((w & 1) << 1)) << 1)) * 16));
#define P1_WO(i_) gblk((const char*)(wo + (size_t)(((i_) * 8 + w) * 8) * 512 + cb * 64), w2o, L_W2 + ((i_) * 8 + w) * 1024)
      static_for<0, 8>([&](auto S) __attribute__((always_inline)) NOPK {
        constexpr int s = decltype(S)::value;
        if constexpr (s == 3) BAR();
        if constexpr (s < 7) { P1_LD(s + 1); }
        WV2(2 * (7 - s) + (s > 4 ? ((s - 4) >> 1) * 4 : 0), ha[0][s], ha[1][s]);
        FENCE();
#pragma unroll
        for (int i = 0; i < 6; ++i)
#pragma unroll
          for (int mt = 0; mt < 2; ++mt) {
            const int sg = i >> 1, hb = i & 1;
            if (sg < 2) acc[sg][hb][mt] = mfma32(wf[s & 1][i], __builtin_bit_cast(bf16x8, ha[mt][s]), acc[sg][hb][mt]);
            else acc[sg][hb][mt] = mfma32(__builtin_bit_cast(bf16x8, ha[mt][s]), wf[s & 1][i], acc[sg][hb][mt]);
          }
        FENCE();
        if constexpr (s >= 4) { if (kh == (s & 1)) { constexpr int q0 = ((s - 4) >> 1) * 4; P1_WO(q0); P1_WO(q0 + 1); P1_WO(q0 + 2); P1_WO(q0 + 3); } }
      });
    }
#pragma unroll
    for (int i = 0; i < 4; ++i) GLD16(tv[i], rsb + (size_t)i * 16 * rpitch, rso, 0);
    MODLD(m2, (unsigned)(1536 * 8 + tid * 16));
    MODLD(gv, gvo);
    BAR();
    TS(6);
#pragma unroll
    for (int s = 0; s < 3; ++s)
#pragma unroll
      for (int mt = 0; mt < 2; ++mt) *(f32x4*)(sm + (w * 6 + s * 2 + mt) * 1024 + l16) = kh ? acc[s][0][mt] : acc[s][1][mt];
    BAR();
    f32x4 t[3][2];
#pragma unroll
    for (int s = 0; s < 3; ++s)
#pragma unroll
      for (int mt = 0; mt < 2; ++mt) {
        const f32x4 o = *(const f32x4*)(sm + ((w ^ 4) * 6 + s * 2 + mt) * 1024 + l16);
        t[s][mt] = (kh ? acc[s][1][mt] : acc[s][0][mt]) + o;
      }
    TS(7);
    const bf16x4 wpT = __builtin_bit_cast(bf16x4, u32x2{wp0 | (wp1 << 16), wp2 | (wp3 << 16)});
    bf16x4 ident;
#pragma unroll
    for (int jj = 0; jj < 4; ++jj) ident[jj] = (4 * gq + jj == m) ? (short)0x3f80 : (short)0;
    bf16x4 pkp[2], pkn[2];
#pragma unroll
    for (int mt = 0; mt < 2; ++mt) {
      bf16x4 pos, neg;
      featmap(mfma16(wpT, pack4(t[1][mt]), z4), pos, neg);
      pkp[mt] = pack4(mfma16(pos, ident, z4));
      pkn[mt] = pack4(mfma16(neg, ident, z4));
    }
    const bf16x8 vf = cat8(pack4(t[2][0]), pack4(t[2][1]));
    const bf16x8 fp = cat8(pkp[0], pkp[1]), fn = cat8(pkn[0], pkn[1]);
    const bf16x8 ones = {0x3f80, 0x3f80, 0x3f80, 0x3f80, 0x3f80, 0x3f80, 0x3f80, 0x3f80};
    f32x4 part[4];
    part[0] = mfma32(fp, vf, z4); part[1] = mfma32(fn, vf, z4); part[2] = mfma32(fp, ones, z4); part[3] = mfma32(fn, ones, z4);
    BAR();
#pragma unroll
    for (int i = 0; i < 4; ++i) *(f32x4*)(sm + (w * 4 + i) * 1024 + l16) = part[i];
    BAR();
    TS(8);
    {
      const rsrc_t rkv = mkrsrc(ws + WS_KVP, 16 * 32 * KVP_STRIDE);
      f32x4 s = z4;
#pragma unroll
      for (int jj = 0; jj < 4; ++jj) s += *(const f32x4*)(sm + ((kh * 4 + jj) * 4 + wr) * 1024 + l16);
      const unsigned base = (unsigned)((r * 32 + head) * KVP_STRIDE);
      if (wr < 2) st16_sc1(rkv, base + wr * 1024 + l16, __builtin_bit_cast(u32x4, s));
      else if (m == 0) st16_sc1(rkv, base + 2048 + (wr - 2) * 64 + gq * 16, __builtin_bit_cast(u32x4, s));
    }
    signal(flg + F_S2 + g * 16 + r, tag);
    TS(9);
    {
      asm volatile("" : "+v"(tv[0]), "+v"(tv[1]), "+v"(tv[2]), "+v"(tv[3]));
      WV4(0, m2[0], m2[1], m2[2], m2[3]); WV4(0, gv[0], gv[1], gv[2], gv[3]);
      const unsigned tl = (unsigned)tag;
      unsigned spins = 0;
      while (true) {
        bool ok = true;
#pragma unroll
        for (int k = 0; k < 4; ++k) ok = ok && m2[k][1] == tl && m2[k][3] == tl && gv[k][1] == tl && gv[k][3] == tl;
        if (__builtin_amdgcn_ballot_w64(!ok) == 0ull || ++spins > (1u << 18)) break;
        __builtin_amdgcn_s_sleep(1);
        MODLD(m2, (unsigned)(1536 * 8 + tid * 16)); MODLD(gv, gvo);
        WV4(0, m2[0], m2[1], m2[2], m2[3]); WV4(0, gv[0], gv[1], gv[2], gv[3]);
      }
      f32x2 mv2 = modsum(m2);
      if (tid >= 256) { mv2[0] += 1.f; mv2[1] += 1.f; }
      *(f32x2*)(sm + L_M2V + tid * 8) = mv2;
      if (w == 0) *(f32x2*)(sm + L_GV + lane * 8) = modsum(gv);
      char* lw = sm + w * 4096;
#pragma unroll
      for (int i = 0; i < 4; ++i) {
        const int rl = i * 8 + (lane >> 3);
        const int sw = ((rl >> 1) & 1) | (((rl >> 3) & 1) << 1);
        *(u32x4*)(lw + rl * 128 + (((lane & 7) ^ (sw << 1)) * 16)) = tv[i];
      }
      BAR();
#pragma unroll
      for (int nt = 0; nt < 2; ++nt) { gav[nt] = LDV4(sm + L_GV + (kh * 32 + nt * 16 + gq * 4) * 4); gbv[nt] = LDV4(sm + L_GV + 256 + (kh * 32 + nt * 16 + gq * 4) * 4); }
      if (nvalid > 0) {
        const int q4 = (lane & 15) >> 2, p4 = lane & 3;
        const int sw = (q4 >> 1) | ((gq & 1) << 1);
        const rsrc_t rwt = mkrsrc(ws + WS_WT, 6 << 20);
        bf16x8 f[4];
#pragma unroll
        for (int nt = 0; nt < 4; ++nt) f[nt] = trfrag(lw + (gq * 8 + q4) * 128 + ((nt ^ sw) * 32) + p4 * 8, 512);
        if (risw3) {
          const char* a_ = sm + L_M2V + (rks * 32 + gq * 8) * 4;
          const f32x4 s0 = LDV4(a_ + 2048), s1 = LDV4(a_ + 2064), b0 = LDV4(a_), b1 = LDV4(a_ + 16);
          u32x4 hi = {pack2(b0[0], b0[1]), pack2(b0[2], b0[3]), pack2(b1[0], b1[1]), pack2(b1[2], b1[3])};
          u32x4 lo = {pack2(b0[0] - lo16(hi[0]), b0[1] - hi16(hi[0])), pack2(b0[2] - lo16(hi[1]), b0[3] - hi16(hi[1])),
                      pack2(b1[0] - lo16(hi[2]), b1[1] - hi16(hi[2])), pack2(b1[2] - lo16(hi[3]), b1[3] - hi16(hi[3]))};
          const u32x4 zz = {0, 0, 0, 0};
          const bf16x8 sbA = __builtin_bit_cast(bf16x8, m == 0 ? hi : (m == 1 ? lo : zz));
#pragma unroll
          for (int nt = 0; nt < 4; ++nt) {
            const f32x4 d2 = mfma32(sbA, f[nt], z4);
            const u32x4 fv = __builtin_bit_cast(u32x4, f[nt]);
            const u32x4 fs = {pack2(lo16(fv[0]) * s0[0], hi16(fv[0]) * s0[1]), pack2(lo16(fv[1]) * s0[2], hi16(fv[1]) * s0[3]),
                              pack2(lo16(fv[2]) * s1[0], hi16(fv[2]) * s1[1]), pack2(lo16(fv[3]) * s1[2], hi16(fv[3]) * s1[3])};
            f[nt] = __builtin_bit_cast(bf16x8, fs);
            const f32x4 d1 = mfma32(ones, f[nt], z4);
            if (nt < nvalid && gq == 0) st8_sc1(rwt, rup + (unsigned)(nt * 16 + m) * 8, u32x2{F2U(d1[0]), F2U(d2[0] + d2[1])});
          }
        }
#pragma unroll
        for (int nt = 0; nt < 4; ++nt) if (nt < nvalid) st16_sc1(rwt, rdst + nt * rstep + l16, __builtin_bit_cast(u32x4, f[nt]));
      }
      WAITV(0);
    }
    TS(10);
    waitflags(flg + F_S2 + g * 16, 16, tag, w, lane);
    if (tid == 0) stflag(flg + F_RT + b, tag);
    TS(11);
    {
      u32x4 pl[4][2]; u32x2 zl;
      const char* kb = ws + WS_KVP + (size_t)((wr * 4) * 32 + head) * KVP_STRIDE;
#pragma unroll
      for (int i = 0; i < 4; ++i) {
        const char* kbi = kb + (size_t)i * 32 * KVP_STRIDE;
        GLD16S(pl[i][0], kbi, l16, 0); GLD16S(pl[i][1], kbi, l16, 1024);
      }
      GLD8S(zl, kb, (unsigned)(gq * 32 * KVP_STRIDE + m * 8), 2048);
#pragma unroll
      for (int mt = 0; mt < 2; ++mt) {
        bf16x4 pos, neg;
        featmap(mfma16(wpT, pack4(t[0][mt]), z4), pos, neg);
        pq[mt] = cat8(pos, neg);
      }
      asm volatile("" : "+v"(pq[0]), "+v"(pq[1]));
      FENCE();
      {
        const int q4 = (lane & 15) >> 2, p4 = lane & 3;
        const int sw = (q4 >> 1) | ((gq & 1) << 1);
#pragma unroll
        for (int nt = 0; nt < 4; ++nt) {
          const char* fb = sm + L_W2 + kh * 32768 + (gq * 8 + q4) * 128 + ((nt ^ sw) * 32) + p4 * 8;
#pragma unroll
          for (int s = 0; s < 8; ++s) w2f[s][nt] = trfrag(fb + s * 4096, 512);
        }
      }
      WV4(0, pl[0][0], pl[0][1], pl[1][0], pl[1][1]); WV4(0, pl[2][0], pl[2][1], pl[3][0], pl[3][1]); WV1(0, zl);
#pragma unroll
      for (int k = 0; k < 2; ++k)
        *(f32x4*)(sm + 65536 + (w * 2 + k) * 1024 + l16) =
            (__builtin_bit_cast(f32x4, pl[0][k]) + __builtin_bit_cast(f32x4, pl[1][k])) + (__builtin_bit_cast(f32x4, pl[2][k]) + __builtin_bit_cast(f32x4, pl[3][k]));
      const float z0 = xsum32(xsum16(U2F(zl[0]))), z1 = xsum32(xsum16(U2F(zl[1])));
      if (gq == 0) *(f32x2*)(sm + w * 128 + m * 8) = f32x2{z0, z1};
    }
    BAR();
    TS(12);
    f32x4 tt[4] = {z4, z4, z4, z4};
#pragma unroll
    for (int jj = 0; jj < 4; ++jj) {
      tt[0] += *(const f32x4*)(sm + 65536 + ((kh * 4 + jj) * 2 + 0) * 1024 + l16);
      tt[1] += *(const f32x4*)(sm + 65536 + ((kh * 4 + jj) * 2 + 1) * 1024 + l16);
      tt[2] += *(const f32x4*)(sm + ((kh * 4 + jj) * 2 + 0) * 64 + gq * 16);
      tt[3] += *(const f32x4*)(sm + ((kh * 4 + jj) * 2 + 1) * 64 + gq * 16);
      if (jj == 1) FENCE();
    }
    const bf16x8 kvA = cat8(pack4(tt[0]), pack4(tt[1])), zA = cat8(pack4(tt[2]), pack4(tt[3]));
    const rsrc_t ro = mkrsrc(ws + WS_O, 2 << 20);
    u32x4 rtf;
    GLD16S(rtf, flg + F_RT + w * 32, (unsigned)((lane & 15) * 16), 0);
#pragma unroll
    for (int mt = 0; mt < 2; ++mt) {
      f32x4 num = mfma32(kvA, pq[mt], z4), den = mfma32(zA, pq[mt], z4);
      const float inv = 1.f / (den[0] + 1e-6f);
      num *= inv;
      const int mtile = r * 8 + wr * 2 + mt;
      st8_sc1(ro, (unsigned)((mtile * 16 + g) * 1024 + (m + 16 * (2 * kh + (gq >> 1))) * 16 + (gq & 1) * 8), __builtin_bit_cast(u32x2, pack4(num)));
    }
    TS(13);
    {
      WV1(0, rtf);
      const unsigned tl = (unsigned)tag, th = (unsigned)(tag >> 32);
      const bool ok = rtf[0] == tl && rtf[1] == th && rtf[2] == tl && rtf[3] == th;
      if (__builtin_amdgcn_ballot_w64(!ok) != 0ull) spinflags(flg + F_RT + w * 32, 32, tag, lane);
    }
    signal(flg + F_S3 + r * 16 + j, tag);
    TS(14);
  }
  u32x2 x1p[2];
  const int mtile2 = r * 8 + rh * 4 + wr, row2 = mtile2 * 16 + m;
  {
    u32x2 xv[2]; float x1v[8];
    if (w >= 2) {
#pragma unroll
      for (int i = 0; i < 12; ++i) w3_piece(wt, l16, t0, nf5, i >> 2, (w - 2) * 4 + (i & 3));
    }
    waitflags(flg + F_S3 + r * 16, 16, tag, w, lane);
    TS(15);
    u32x4 af[8];
    const char* ob = ws + WS_O + (size_t)(mtile2 * 16 + kh * 8) * 1024;
#pragma unroll
    for (int ks = 0; ks < 4; ++ks) GLD16S(af[ks], ob, l16, ks * 1024);
#pragma unroll
    for (int ks = 4; ks < 8; ++ks) GLD16S(af[ks], ob + 4096, l16, (ks - 4) * 1024);
    GLD8(xv[0], x, (unsigned)(row2 * 1024 + n2 * 2), 0); GLD8(xv[1], x, (unsigned)(row2 * 1024 + n2 * 2), 32);
    f32x4 acc[4] = {z4, z4, z4, z4};
#pragma unroll
    for (int ks = 0; ks < 8; ++ks) {
      WV1(7 - ks + 2, af[ks]);
      FENCE();
#pragma unroll
      for (int nt = 0; nt < 4; ++nt) acc[nt] = mfma32(w2f[ks][nt], __builtin_bit_cast(bf16x8, af[ks]), acc[nt]);
      FENCE();
    }
#pragma unroll
    for (int nt = 0; nt < 2; ++nt) *(f32x4*)(sm + L_TOP + (w * 2 + nt) * 1024 + l16) = kh ? acc[nt] : acc[2 + nt];
    WV2(0, xv[0], xv[1]);
    BAR();
    const rsrc_t rx1 = mkrsrc(ws + WS_X1, 2 << 20), rst = mkrsrc(ws + WS_ST2, 256 << 10);
#pragma unroll
    for (int nt = 0; nt < 2; ++nt) {
      const f32x4 a = (kh ? acc[2 + nt] : acc[nt]) + *(const f32x4*)(sm + L_TOP + ((w ^ 4) * 2 + nt) * 1024 + l16);
      const f32x4 gaf = gav[nt];
      x1v[nt * 4 + 0] = lo16(xv[nt][0]) + gaf[0] * a[0]; x1v[nt * 4 + 1] = hi16(xv[nt][0]) + gaf[1] * a[1];
      x1v[nt * 4 + 2] = lo16(xv[nt][1]) + gaf[2] * a[2]; x1v[nt * 4 + 3] = hi16(xv[nt][1]) + gaf[3] * a[3];
      u32x2 ov = {pack2(x1v[nt * 4], x1v[nt * 4 + 1]), pack2(x1v[nt * 4 + 2], x1v[nt * 4 + 3])};
      x1p[nt] = ov;
      x1v[nt * 4 + 0] = lo16(ov[0]); x1v[nt * 4 + 1] = hi16(ov[0]); x1v[nt * 4 + 2] = lo16(ov[1]); x1v[nt * 4 + 3] = hi16(ov[1]);
      st8_sc1(rx1, (unsigned)((mtile2 * 16 + cb * 2 + kh) * 1024 + (m + 16 * (2 * nt + (gq >> 1))) * 16 + (gq & 1) * 8), ov);
    }
    float s = ((x1v[0] + x1v[1]) + (x1v[2] + x1v[3])) + ((x1v[4] + x1v[5]) + (x1v[6] + x1v[7]));
    s = xsum32(xsum16(s));
    const float mean = s * (1.f / 32.f);
    float q2 = 0;
#pragma unroll
    for (int jj = 0; jj < 8; ++jj) { float d = x1v[jj] - mean; q2 += d * d; }
    q2 = xsum32(xsum16(q2));
    if (gq == 0) { f32x2 st = {mean, q2}; st8_sc1(rst, (unsigned)(row2 * 128 + (cb * 2 + kh) * 8), __builtin_bit_cast(u32x2, st)); }
    TS(16);
    signal(flg + F_S4 + r * 16 + j, tag);
    if (w >= 4) {
#pragma unroll
      for (int i = 0; i < 18; ++i) w3_piece(wt, l16, t0, nf5, 3 + i / 6, (w - 4) * 6 + i % 6);
    } else if (w >= 1) {
      const int t = tid - 64, mat = t / 96; int cc = t % 96; if (nf5 && cc >= 80) cc -= 16;
      const unsigned uo = (unsigned)((mat * 16 * 1376 + t0 * 16 + cc) * 8);
      u32x2 uv[16];
#pragma unroll
      for (int k = 0; k < 16; ++k) GLD8S(uv[k], wt + WT_UP + k * 11008, uo, 0);
      WV4(0, uv[0], uv[1], uv[2], uv[3]); WV4(0, uv[4], uv[5], uv[6], uv[7]); WV4(0, uv[8], uv[9], uv[10], uv[11]); WV4(0, uv[12], uv[13], uv[14], uv[15]);
      float u1 = 0, u2 = 0;
#pragma unroll
      for (int k = 0; k < 16; ++k) { u1 += U2F(uv[k][0]); u2 += U2F(uv[k][1]); }
      ((float*)(sm + L_TOP))[t] = u1; ((float*)(sm + L_TOP + 768))[t] = u2;
    }
    waitflags(flg + F_S4 + r * 16, 16, tag, w, lane);
    TS(17);
  }
  if (j < 6) phase3<6>(sm, ws, r, t0, cb, w, lane, tag TSPASS);
  else phase3<5>(sm, ws, r, t0, cb, w, lane, tag TSPASS);
  {
    if (w >= 4) {
#pragma unroll
      for (int i = 12; i < 20; ++i) { w4_piece(wt, l16, cb, i, (w - 4) * 2); w4_piece(wt, l16, cb, i, (w - 4) * 2 + 1); }
    }
    const LB fb = mklb(kh * 4096 + l16);
    bf16x8 wf[4][4];
#pragma unroll
    for (int s = 0; s < 3; ++s)
#pragma unroll
      for (int nt = 0; nt < 4; ++nt) wf[s][nt] = LDB(fb, w4_off(s) + nt * 1024);
    waitflags(flg + F_S5 + r * 16, 16, tag, w, lane);
    TS(23);
    u32x4 af[22];
    const char* hb0 = ws + WS_HH + (size_t)(mtile2 * 43 + kh * 22) * 1024;
#pragma unroll
    for (int i = 0; i < 21; ++i) GLD16S(af[i], hb0 + (i >> 2) * 4096, l16, (i & 3) * 1024);
    { const char* hl = hb0 + (kh ? 20 : 21) * 1024; GLD16S(af[21], hl, l16, 0); }
    w4_piece(wt, l16, cb, 20, w); w4_piece(wt, l16, cb, 21, w);
    f32x4 acc[4] = {z4, z4, z4, z4};
#pragma unroll
    for (int i = 0; i < 22; ++i) {
      if (i == 8) BAR();
      if (i == 17) { WVN(0); BAR(); }
      if (i + 3 < 22) {
#pragma unroll
        for (int nt = 0; nt < 4; ++nt) wf[(i + 3) & 3][nt] = LDB(fb, w4_off(i + 3) + nt * 1024);
      }
      WV1((21 - i) + 2, af[i]);
      FENCE();
      if (i < 21 || kh == 0) {
#pragma unroll
        for (int nt = 0; nt < 4; ++nt) acc[nt] = mfma32(wf[i & 3][nt], __builtin_bit_cast(bf16x8, af[i]), acc[nt]);
      }
      FENCE();
    }
    TS(25);
    BAR();
#pragma unroll
    for (int nt = 0; nt < 2; ++nt) *(f32x4*)(sm + (w * 2 + nt) * 1024 + l16) = kh ? acc[nt] : acc[2 + nt];
    BAR();
#pragma unroll
    for (int nt = 0; nt < 2; ++nt) {
      const f32x4 a = (kh ? acc[2 + nt] : acc[nt]) + *(const f32x4*)(sm + ((w ^ 4) * 2 + nt) * 1024 + l16);
      const f32x4 gbf = gbv[nt];
      u32x2 ov = {pack2(lo16(x1p[nt][0]) + gbf[0] * a[0], hi16(x1p[nt][0]) + gbf[1] * a[1]), pack2(lo16(x1p[nt][1]) + gbf[2] * a[2], hi16(x1p[nt][1]) + gbf[3] * a[3])};
      *(u32x2*)(out + (size_t)row2 * 512 + n2 + nt * 16) = ov;
    }
    if (b == 0 && tid == 0) stflag(flg + F_E, tag);
    TS(26);
    STAMP_FLUSH(1, ws);
  }
}

extern "C" void kernel_launch(void* const* d_in, const int* in_sizes, int n_in, void* d_out, int out_size, void* d_ws, size_t ws_size,
                              hipStream_t stream) {
  const unsigned short* x = (const unsigned short*)d_in[0];
  const unsigned short* wqkv = (const unsigned short*)d_in[1];
  const unsigned short* wphi = (const unsigned short*)d_in[2];
  const unsigned short* wo = (const unsigned short*)d_in[3];
  const unsigned short* wg = (const unsigned short*)d_in[5];
  const unsigned short* wu = (const unsigned short*)d_in[6];
  const unsigned short* wd = (const unsigned short*)d_in[7];
  const unsigned short* c = (const unsigned short*)d_in[8];
  const unsigned short* wm = (const unsigned short*)d_in[9];
  char* ws = (char*)d_ws;
  (void)hipFuncSetAttribute((const void*)k1_main, hipFuncAttributeMaxDynamicSharedMemorySize, 163840);
  k1_main<<<256, 512, 163840, stream>>>(x, wqkv, wphi, wo, wg, wu, wd, c, wm, ws, (unsigned short*)d_out);
}
```

```cpp
#include <hip/hip_runtime.h>
#include <hip/hip_bf16.h>
#include <stdint.h>

typedef __attribute__((ext_vector_type(8))) short bf16x8;
typedef __attribute__((ext_vector_type(4))) short bf16x4;
typedef __attribute__((ext_vector_type(4))) float f32x4;
typedef __attribute__((ext_vector_type(2))) float f32x2;
typedef __attribute__((ext_vector_type(2))) __bf16 bf2_t;
typedef __attribute__((ext_vector_type(4))) unsigned u32x4;
typedef __attribute__((ext_vector_type(2))) unsigned u32x2;
#define LAS __attribute__((address_space(3)))
#define U2F(x) __builtin_bit_cast(float, (unsigned)(x))
#define F2U(x) __builtin_bit_cast(unsigned, (float)(x))
#if defined(__HIP_DEVICE_COMPILE__) && __HIP_DEVICE_COMPILE__
#define NOPK __attribute__((target("no-packed-fp32-ops")))
#else
#define NOPK
#endif
#define DEVI __device__ __forceinline__ NOPK

DEVI unsigned pack2(float a, float b) { f32x2 p = {a, b}; bf2_t q = __builtin_convertvector(p, bf2_t); return __builtin_bit_cast(unsigned, q); }
DEVI float lo16(unsigned u) { return U2F(u << 16); }
DEVI float hi16(unsigned u) { return U2F(u & 0xffff0000u); }
DEVI float vmax(float a, float b) { return __builtin_amdgcn_fmed3f(a, b, __builtin_inff()); }
DEVI float vmin(float a, float b) { return __builtin_amdgcn_fmed3f(a, b, -__builtin_inff()); }
DEVI float vmax3(float a, float b, float c) { return vmax(vmax(a, b), c); }
DEVI float vmin3(float a, float b, float c) { return vmin(vmin(a, b), c); }
DEVI float xmax16(float v) { auto r = __builtin_amdgcn_permlane16_swap(F2U(v), F2U(v), false, false); return vmax(U2F(r[0]), U2F(r[1])); }
DEVI float xmax32(float v) { auto r = __builtin_amdgcn_permlane32_swap(F2U(v), F2U(v), false, false); return vmax(U2F(r[0]), U2F(r[1])); }
DEVI float xmin16(float v) { auto r = __builtin_amdgcn_permlane16_swap(F2U(v), F2U(v), false, false); return vmin(U2F(r[0]), U2F(r[1])); }
DEVI float xmin32(float v) { auto r = __builtin_amdgcn_permlane32_swap(F2U(v), F2U(v), false, false); return vmin(U2F(r[0]), U2F(r[1])); }
DEVI float xsum16(float v) { auto r = __builtin_amdgcn_permlane16_swap(F2U(v), F2U(v), false, false); return U2F(r[0]) + U2F(r[1]); }
DEVI float xsum32(float v) { auto r = __builtin_amdgcn_permlane32_swap(F2U(v), F2U(v), false, false); return U2F(r[0]) + U2F(r[1]); }
template <int CTRL> DEVI float dpp(float v) { return U2F(__builtin_amdgcn_update_dpp(0u, F2U(v), CTRL, 0xf, 0xf, true)); }
DEVI float xsum1(float v) { return v + dpp<0xB1>(v); }
DEVI float xsum2(float v) { return v + dpp<0x4E>(v); }
DEVI float fexp2(float x) { return __builtin_amdgcn_exp2f(x); }
DEVI float frcp(float x) { return __builtin_amdgcn_rcpf(x); }
DEVI f32x4 mfma32(bf16x8 a, bf16x8 b, f32x4 c) { return __builtin_amdgcn_mfma_f32_16x16x32_bf16(a, b, c, 0, 0, 0); }
DEVI f32x4 mfma16(bf16x4 a, bf16x4 b, f32x4 c) { return __builtin_amdgcn_mfma_f32_16x16x16bf16_1k(a, b, c, 0, 0, 0); }
DEVI bf16x4 pack4(f32x4 v) { u32x2 u = {pack2(v[0], v[1]), pack2(v[2], v[3])}; return __builtin_bit_cast(bf16x4, u); }
DEVI bf16x8 cat8(bf16x4 a, bf16x4 b) { return __builtin_shufflevector(a, b, 0, 1, 2, 3, 4, 5, 6, 7); }
DEVI void glds16(const void* g, unsigned l) {
  unsigned keep;
  asm volatile("s_mov_b32 %0, m0\n\ts_mov_b32 m0, %2\n\ts_nop 0\n\tglobal_load_lds_dwordx4 %1, off\n\ts_mov_b32 m0, %0" : "=&s"(keep) : "v"(g), "s"(l) : "memory");
}
DEVI bf16x4 trread(const char* lds) { return __builtin_amdgcn_ds_read_tr16_b64_v4i16((LAS bf16x4*)lds); }
DEVI bf16x8 trfrag(const char* lds, int step) { return cat8(trread(lds), trread(lds + step)); }
#define WAITV(n) asm volatile("s_waitcnt vmcnt(" #n ")" ::: "memory")
#define BAR() do { asm volatile("s_waitcnt lgkmcnt(0)" ::: "memory"); __builtin_amdgcn_s_barrier(); asm volatile("" ::: "memory"); } while (0)
#define STAMP_DECL
#define TS(i)
#define STAMP_FLUSH(K, wsbase)
#define TSARG
#define TSPASS
#define FENCE() __builtin_amdgcn_sched_barrier(0)
template <int I> struct IC { static constexpr int value = I; };
template <int I, int N, class F> DEVI void static_for(F&& f) { if constexpr (I < N) { f(IC<I>{}); static_for<I + 1, N>(f); } }
typedef unsigned long long u64;
constexpr size_t WS_FLG = 0;
constexpr int F_E = 0, F_S1 = 16, F_S2 = 16 * 17, F_S3 = 16 * 33, F_S4 = 16 * 49, F_S5 = 16 * 65, F_RT = 16 * 81, F_S5B = 16 * 97;
constexpr size_t WS_MODP = 64 << 10;
constexpr size_t WS_H = 1 << 20;
constexpr size_t WS_KVP = 4 << 20;
constexpr size_t WS_O = 8 << 20;
constexpr size_t WS_X1 = 12 << 20;
constexpr size_t WS_ST2 = 16 << 20;
constexpr size_t WS_HH = 20 << 20;
constexpr size_t WS_WT = 32 << 20;
constexpr unsigned WT_UP = 0;
constexpr unsigned WT_W3 = 1 << 20;
constexpr unsigned WT_W4 = 4 << 20;
constexpr int KVP_STRIDE = 2304;

#define GLD16(dst, sbase, voff, imm) asm volatile("global_load_dwordx4 %0, %1, %2 offset:%3" : "=v"(dst) : "v"(voff), "s"(sbase), "i"(imm) : "memory")
#define GLD16S(dst, sbase, voff, imm) asm volatile("global_load_dwordx4 %0, %1, %2 offset:%3 sc1" : "=v"(dst) : "v"(voff), "s"(sbase), "i"(imm) : "memory")
#define GLD8(dst, sbase, voff, imm) asm volatile("global_load_dwordx2 %0, %1, %2 offset:%3" : "=v"(dst) : "v"(voff), "s"(sbase), "i"(imm) : "memory")
#define GLD8S(dst, sbase, voff, imm) asm volatile("global_load_dwordx2 %0, %1, %2 offset:%3 sc1" : "=v"(dst) : "v"(voff), "s"(sbase), "i"(imm) : "memory")
#define GLDU16(dst, sbase, voff, imm) asm volatile("global_load_ushort %0, %1, %2 offset:%3" : "=v"(dst) : "v"(voff), "s"(sbase), "i"(imm) : "memory")
#define WV1(n, a) asm volatile("s_waitcnt vmcnt(%1)" : "+v"(a) : "i"(n) : "memory")
#define WV2(n, a, b) asm volatile("s_waitcnt vmcnt(%2)" : "+v"(a), "+v"(b) : "i"(n) : "memory")
#define WV4(n, a, b, c, d) asm volatile("s_waitcnt vmcnt(%4)" : "+v"(a), "+v"(b), "+v"(c), "+v"(d) : "i"(n) : "memory")
#define WVN(n) asm volatile("s_waitcnt vmcnt(%0)" :: "i"(n) : "memory")
DEVI void gblk(const void* sbase, unsigned voff, unsigned l) {
  unsigned keep;
  asm volatile("s_mov_b32 %0, m0\n\ts_mov_b32 m0, %3\n\ts_nop 0\n\tglobal_load_lds_dwordx4 %1, %2\n\ts_mov_b32 m0, %0" : "=&s"(keep) : "v"(voff), "s"(sbase), "s"(l) : "memory");
}
DEVI void gblks(const void* sbase, unsigned voff, unsigned l) {
  unsigned keep;
  asm volatile("s_mov_b32 %0, m0\n\ts_mov_b32 m0, %3\n\ts_nop 0\n\tglobal_load_lds_dwordx4 %1, %2 sc1\n\ts_mov_b32 m0, %0" : "=&s"(keep) : "v"(voff), "s"(sbase), "s"(l) : "memory");
}

DEVI void featmap(f32x4 p, bf16x4& pos, bf16x4& neg) {
  constexpr float L2E = 1.4426950408889634f;
  float mx = vmax(vmax(p[0], p[1]), vmax(p[2], p[3]));
  float mn = vmin(vmin(p[0], p[1]), vmin(p[2], p[3]));
  mx = xmax32(xmax16(mx)); mn = xmin32(xmin16(mn));
  f32x4 ep, en;
#pragma unroll
  for (int j = 0; j < 4; ++j) { ep[j] = fexp2((p[j] - mx) * L2E); en[j] = fexp2((mn - p[j]) * L2E); }
  float sp = (ep[0] + ep[1]) + (ep[2] + ep[3]), sn = (en[0] + en[1]) + (en[2] + en[3]);
  sp = xsum32(xsum16(sp)); sn = xsum32(xsum16(sn));
  const float ip = frcp(sp), in = frcp(sn);
  ep *= ip; en *= in;
  pos = pack4(ep); neg = pack4(en);
}

typedef __amdgpu_buffer_rsrc_t rsrc_t;
DEVI rsrc_t mkrsrc(const void* p, unsigned bytes) { return __builtin_amdgcn_make_buffer_rsrc((void*)p, 0, bytes, 0x00020000); }
DEVI void st16_sc1(rsrc_t r, unsigned off, u32x4 v) { __builtin_amdgcn_raw_buffer_store_b128(v, r, off, 0, 16); }
DEVI void st8_sc1(rsrc_t r, unsigned off, u32x2 v) { __builtin_amdgcn_raw_buffer_store_b64(v, r, off, 0, 16); }
DEVI void st4_sc1(rsrc_t r, unsigned off, unsigned v) { __builtin_amdgcn_raw_buffer_store_b32(v, r, off, 0, 16); }
DEVI u64 ldflag(const u64* p) { return __hip_atomic_load(p, __ATOMIC_RELAXED, __HIP_MEMORY_SCOPE_AGENT); }
DEVI void stflag(u64* p, u64 v) { __hip_atomic_store(p, v, __ATOMIC_RELAXED, __HIP_MEMORY_SCOPE_AGENT); }

DEVI void signal(u64* f, u64 tag) {
  WAITV(0);
  BAR();
  if (threadIdx.x == 0) stflag(f, tag);
}
DEVI void spinflags(const u64* f, int n, u64 tag, int lane) {
  const u64* p = f + (lane < n ? lane : 0);
  unsigned spins = 0;
  while (__builtin_amdgcn_ballot_w64(ldflag(p) != tag) != 0ull) {
    __builtin_amdgcn_s_sleep(1);
    if (++spins > (1u << 20)) break;
  }
}
DEVI void waitflags(const u64* f, int n, u64 tag, int w, int lane) {
  if (w == 0) spinflags(f, n, tag, lane);
  BAR();
}

constexpr int SLOT = 24576, L_TOP = 147456;
constexpr int L_W2 = 98304;
constexpr int L_SCR = 147456, L_MOD1 = 149504, L_HT = 153600;
constexpr int L_M2V = 49152, L_GV = 53248;
DEVI constexpr int w1_off(int c) { return c * SLOT; }
DEVI constexpr int w3_slot(int ks) { return (ks % 6) * SLOT; }
DEVI constexpr int w4_off(int i) { return i < 12 ? 2 * SLOT + i * 8192 : i < 18 ? (i - 12) * 8192 : i < 20 ? L_TOP + (i - 18) * 8192 : 2 * SLOT + (i - 20) * 8192; }
DEVI void w3_piece(const char* wt, unsigned l16, int t0, bool nf5, int st, int id) {
  const int k2 = id / 12, khh = (id / 6) & 1, mat = (id / 3) & 1; int f = (st >> 2) * 3 + id % 3;
  if (nf5 && f == 5) f = 4;
  gblks(wt + WT_W3 + (size_t)((mat * 86 + t0 + f) * 16 + khh * 8 + (st & 3) * 2 + k2) * 1024, l16, w3_slot(st) + id * 1024);
}
DEVI int p4_ks(int khh, int i) { const int k = i < 12 ? khh * 12 + i : 24 + khh * 10 + (i - 12); return k > 42 ? 42 : k; }
DEVI void w4_piece(const char* wt, unsigned l16, int cb, int i, int pc) {
  const int khh = pc >> 2, nt = (pc & 3) ^ (khh * 2);
  gblks(wt + WT_W4 + (size_t)((cb * 4 + nt) * 43 + p4_ks(khh, i)) * 1024, l16, w4_off(i) + pc * 1024);
}
DEVI int pos2ft(int p) {
  int jj, f;
  if (p < 48) { jj = p / 3; f = p % 3; } else { const int q = p - 48; if (q < 18) { jj = q / 3; f = 3 + q % 3; } else { jj = 6 + (q - 18) / 2; f = 3 + (q - 18) % 2; } }
  return (jj < 6 ? jj * 6 : 36 + (jj - 6) * 5) + f;
}

DEVI float sfma(float a, float b, float c) { return __builtin_fmaf(a, b, c); }
DEVI bf16x8 modulate2(u32x4 xv, float a, float c, f32x4 s0, f32x4 s1, f32x4 a0, f32x4 a1) {
  u32x4 o;
  o[0] = pack2(sfma(sfma(lo16(xv[0]), a, c), s0[0], a0[0]), sfma(sfma(hi16(xv[0]), a, c), s0[1], a0[1]));
  o[1] = pack2(sfma(sfma(lo16(xv[1]), a, c), s0[2], a0[2]), sfma(sfma(hi16(xv[1]), a, c), s0[3], a0[3]));
  o[2] = pack2(sfma(sfma(lo16(xv[2]), a, c), s1[0], a1[0]), sfma(sfma(hi16(xv[2]), a, c), s1[1], a1[1]));
  o[3] = pack2(sfma(sfma(lo16(xv[3]), a, c), s1[2], a1[2]), sfma(sfma(hi16(xv[3]), a, c), s1[3], a1[3]));
  return __builtin_bit_cast(bf16x8, o);
}
DEVI float dot2(unsigned a, unsigned b, float c) { return __builtin_amdgcn_fdot2_f32_bf16(__builtin_bit_cast(bf2_t, a), __builtin_bit_cast(bf2_t, b), c, false); }
#define LDF(p) (*(const bf16x8*)(p))
#define LDV4(p) (*(const f32x4*)(p))
struct LB { const LAS char* b[3]; };
DEVI LB mklb(unsigned off) { LB l; unsigned a0 = off, a1 = off + 65536, a2 = off + 131072; asm("" : "+v"(a0)); asm("" : "+v"(a1)); asm("" : "+v"(a2));
  l.b[0] = (const LAS char*)(size_t)a0; l.b[1] = (const LAS char*)(size_t)a1; l.b[2] = (const LAS char*)(size_t)a2; return l; }
#define LDB(lb, off) (*(const LAS bf16x8*)((lb).b[(off) >> 16] + ((off) & 65535)))
#define TRB(lb, off) __builtin_amdgcn_ds_read_tr16_b64_v4i16((LAS bf16x4*)((lb).b[(off) >> 16] + ((off) & 65535)))
#define MIX(n, k) _Pragma("unroll") for (int i_ = 0; i_ < (n); ++i_) { __builtin_amdgcn_sched_group_barrier(0x008, 1, 0); __builtin_amdgcn_sched_group_barrier(0x002, (k), 0); }

DEVI f32x2 rbf2(float a, float b) { const unsigned u = pack2(a, b); return f32x2{lo16(u), hi16(u)}; }
DEVI void gemv_part(const unsigned c2, const u32x4 (&wm)[2], char* scr, int w, int lane) {
  float acc[8];
  const float c0 = lo16(c2), c1 = hi16(c2);
  const f32x2 sr = rbf2(c0 / (1.f + fexp2(c0 * -1.4426950408889634f)), c1 / (1.f + fexp2(c1 * -1.4426950408889634f)));
  const float s0 = sr[0], s1 = sr[1];
#pragma unroll
  for (int j = 0; j < 4; ++j) { acc[2 * j] = sfma(s1, lo16(wm[1][j]), s0 * lo16(wm[0][j])); acc[2 * j + 1] = sfma(s1, hi16(wm[1][j]), s0 * hi16(wm[0][j])); }
#pragma unroll
  for (int j = 0; j < 8; ++j) { float v = acc[j]; v += dpp<0x128>(v); acc[j] = xsum32(xsum16(v)); }
  if (lane < 8) {
    float* d = (float*)scr + w * 64 + lane * 8;
    *(f32x4*)d = f32x4{acc[0], acc[1], acc[2], acc[3]}; *(f32x4*)(d + 4) = f32x4{acc[4], acc[5], acc[6], acc[7]};
  }
}
DEVI f32x2 modsum(const u32x4 (&g)[4]) {
  return rbf2((U2F(g[0][0]) + U2F(g[1][0])) + (U2F(g[2][0]) + U2F(g[3][0])), (U2F(g[0][2]) + U2F(g[1][2])) + (U2F(g[2][2]) + U2F(g[3][2])));
}
#define GLD4(dst, sbase, voff, imm) asm volatile("global_load_dword %0, %1, %2 offset:%3" : "=v"(dst) : "v"(voff), "s"(sbase), "i"(imm) : "memory")
#define MODLD(g, voff) { GLD16S(g[0], modp, voff, 0); GLD16S(g[1], modp + 24576, voff, 0); GLD16S(g[2], modp + 49152, voff, 0); GLD16S(g[3], modp + 73728, voff, 0); }

DEVI u32x4 widen(u32x2 a, u32x2 b) {
  auto r0 = __builtin_amdgcn_permlane16_swap(a[0], b[0], false, false);
  auto r1 = __builtin_amdgcn_permlane16_swap(a[1], b[1], false, false);
  return u32x4{r0[0], r1[0], r0[1], r1[1]};
}

template <int NF>
DEVI void phase3(char* sm, char* ws, int r, int j, int t0, int cb, int w, int lane, u64 tag TSARG) {
  u64* flg = (u64*)(ws + WS_FLG);
  const int wr = w & 3, kh = w >> 2, m = lane & 15, gq = lane >> 4;
  const unsigned l16 = lane * 16;
  const char* wt = ws + WS_WT;
  const rsrc_t rhh = mkrsrc(ws + WS_HH, 128 * 43 * 1024);
  const int mtile = r * 8 + wr * 2 + kh;
  u32x4 xa[2][8], sta[2];
  const char* xb0 = ws + WS_X1 + (size_t)(mtile * 16 + kh * 8) * 1024;
  const char* xb1 = ws + WS_X1 + (size_t)((mtile ^ 1) * 16 + kh * 8) * 1024;
#define P3_X(ks_) { GLD16S(xa[0][ks_], xb0 + ((ks_) >> 2) * 4096, l16, ((ks_) & 3) * 1024); GLD16S(xa[1][ks_], xb1 + ((ks_) >> 2) * 4096, l16, ((ks_) & 3) * 1024); }
  P3_X(0); P3_X(1); P3_X(2); P3_X(3);
  const char* stb = ws + WS_ST2 + (size_t)(mtile * 16) * 128;
  u32x4& sta0 = sta[0]; u32x4& sta1 = sta[1];
  const LB fb = mklb(kh * 6144 + l16);
  bf16x8 wf[3][3];
  f32x4 acc[2][3][2];
  float rs = 0, nb = 0;
#define P3_NT(gi_) (((gi_) >= 16 && NF == 5) ? 2 : 3)
#define P3_LD(gi_) _Pragma("unroll") for (int f3 = 0; f3 < P3_NT(gi_); ++f3) wf[(gi_) % 3][f3] = LDB(fb, w3_slot((gi_) >> 2) + (((gi_) >> 1) & 1) * 12288 + ((gi_) & 1) * 3072 + f3 * 1024);
#define P3_MM(gi_) _Pragma("unroll") for (int f3 = 0; f3 < P3_NT(gi_); ++f3) { constexpr int ks_ = ((gi_) >> 1) & 7; constexpr bool first_ = ks_ == 0; \
    acc[(gi_) & 1][f3][0] = mfma32(wf[(gi_) % 3][f3], __builtin_bit_cast(bf16x8, xa[0][ks_]), first_ ? f32x4{0, 0, 0, 0} : acc[(gi_) & 1][f3][0]); \
    acc[(gi_) & 1][f3][1] = mfma32(wf[(gi_) % 3][f3], __builtin_bit_cast(bf16x8, xa[1][ks_]), first_ ? f32x4{0, 0, 0, 0} : acc[(gi_) & 1][f3][1]); }
  const char* ub = sm + L_TOP + gq * 16;
#define P3_EPI(half_, xoff_) { \
    constexpr int nt_ = ((half_) && NF == 5) ? 2 : 3; \
    char* xw = sm + (xoff_) + w * 6144 + l16; const char* xr = sm + (xoff_) + (w ^ 4) * 6144 + l16; \
    _Pragma("unroll") for (int a = 0; a < 2; ++a) _Pragma("unroll") for (int f3 = 0; f3 < nt_; ++f3) *(f32x4*)(xw + (a * 3 + f3) * 1024) = acc[a][f3][1]; \
    BAR(); \
    u32x2 hp[3]; \
    const int p0 = (half_) ? 48 + (j < 6 ? 3 * j : 18 + 2 * (j - 6)) : 3 * j; \
    _Pragma("unroll") for (int f3 = 0; f3 < nt_; ++f3) { \
      const int f = (half_) * 3 + f3; \
      const f32x4 gv = acc[0][f3][0] + *(const f32x4*)(xr + f3 * 1024), uv = acc[1][f3][0] + *(const f32x4*)(xr + (3 + f3) * 1024); \
      const f32x4 g1 = LDV4(ub + f * 64), g2 = LDV4(ub + 768 + f * 64), u1 = LDV4(ub + 384 + f * 64), u2 = LDV4(ub + 1152 + f * 64); \
      f32x4 hv; \
      _Pragma("unroll") for (int e = 0; e < 4; ++e) { \
        const float xg = sfma(rs, gv[e], sfma(nb, g1[e], g2[e])); \
        const float xu = sfma(rs, uv[e], sfma(nb, u1[e], u2[e])); \
        const float y2 = xg * (-2.302208198f - 0.1029432397f * xg * xg); \
        hv[e] = xg * frcp(1.f + fexp2(y2)) * xu; \
      } \
      hp[f3] = __builtin_bit_cast(u32x2, pack4(hv)); \
      if (f3 >= 1) { \
        const int fa = f3 == 1 ? 0 : 2; \
        const u32x4 wv = widen(hp[fa], hp[f3]); \
        const int pp = p0 + ((gq & 1) ? f3 : fa); \
        if (f3 == 1 || !(gq & 1)) st16_sc1(rhh, (unsigned)(mtile * 43 * 1024 + pp * 512 + (m + 16 * (gq >> 1)) * 16), wv); \
      } \
    } }
  P3_LD(0); P3_LD(1);
  TS(19);
  static_for<0, 32>([&](auto GI) __attribute__((always_inline)) NOPK {
    constexpr int gi = decltype(GI)::value;
    (void)sta0; (void)sta1;
    if constexpr (gi == 6) BAR();
    if constexpr (gi == 16) {
      WV2(6, sta0, sta1);
      const f32x4 a = __builtin_bit_cast(f32x4, sta0), bq = __builtin_bit_cast(f32x4, sta1);
      float sm_ = (a[0] + a[2]) + (bq[0] + bq[2]);
      sm_ = xsum32(xsum16(sm_));
      const float mean = sm_ * (1.f / 16.f);
      float d0 = a[0] - mean, d1 = a[2] - mean, d2 = bq[0] - mean, d3 = bq[2] - mean;
      float q = (a[1] + a[3]) + (bq[1] + bq[3]) + 32.f * ((d0 * d0 + d1 * d1) + (d2 * d2 + d3 * d3));
      q = xsum32(xsum16(q));
      rs = 1.f / __builtin_sqrtf(q * (1.f / 512.f) + 1e-5f);
      nb = -mean * rs;
      BAR();
      P3_EPI(0, 2 * SLOT);
      FENCE();
    }
    if constexpr (gi == 18) BAR();
    if constexpr (gi == 22) { WVN(4); BAR(); if (threadIdx.x == 0) stflag(flg + F_S5 + r * 16 + j, tag); }
    if constexpr (gi + 2 < 32) { P3_LD(gi + 2); }
    if constexpr (gi < 16 && (gi & 1) == 0) {
      constexpr int ks = gi >> 1;
      if constexpr (ks < 4) { P3_X(ks + 4); }
      if constexpr (ks == 2) { _Pragma("unroll") for (int i = 0; i < 3; ++i) w3_piece(wt, l16, t0, NF == 5, 5, w * 3 + i); }
      if constexpr (ks == 3) { const unsigned so = (unsigned)(m * 128 + gq * 32); GLD16S(sta0, stb, so, 0); GLD16S(sta1, stb, so, 16); }
      constexpr int pa = gi > 6 ? (gi < 12 ? ((gi - 6) >> 1) * 2 : 6) : 0;
      constexpr int ny = ks == 0 ? 8 : ks == 1 ? 8 : ks == 2 ? 11 : ks == 3 ? 13 : ks == 4 ? 11 + pa : ks == 5 ? 9 + pa : ks == 6 ? 7 + pa : 2 + pa;
      WV2(ny, xa[0][ks], xa[1][ks]);
    }
    FENCE();
    P3_MM(gi);
    FENCE();
    if constexpr (gi >= 6 && gi < 12) { if (kh == (gi & 1)) { constexpr int q = ((gi - 6) >> 1) * 2; w3_piece(wt, l16, t0, NF == 5, 6 + q / 3, w * 3 + q % 3); w3_piece(wt, l16, t0, NF == 5, 6 + (q + 1) / 3, w * 3 + (q + 1) % 3); } }
    if constexpr (gi >= 18 && gi < 30) { if (kh == (gi & 1)) { constexpr int q = ((gi - 18) >> 1) * 2; w4_piece(wt, l16, cb, q, w); w4_piece(wt, l16, cb, q + 1, w); } }
  });
  TS(20);
  BAR();
  P3_EPI(1, 0);
  TS(21);
  signal(flg + F_S5B + r * 16 + j, tag);
  TS(22);
}

__global__ __launch_bounds__(512) __attribute__((amdgpu_waves_per_eu(2, 2))) NOPK
void k1_main(const unsigned short* __restrict__ x, const unsigned short* __restrict__ wqkv, const unsigned short* __restrict__ wphi,
             const unsigned short* __restrict__ wo, const unsigned short* __restrict__ wg, const unsigned short* __restrict__ wu,
             const unsigned short* __restrict__ wd, const unsigned short* __restrict__ cvec, const unsigned short* __restrict__ wmod,
             char* __restrict__ ws, unsigned short* __restrict__ out) {
  extern __shared__ __attribute__((aligned(16))) char sm[];
  STAMP_DECL TS(0);
  const int tid = threadIdx.x, lane = tid & 63, w = __builtin_amdgcn_readfirstlane(tid >> 6);
  const int b = blockIdx.x, xcd = b & 7, idx = b >> 3;
  const int r = xcd * 2 + (idx >> 4), j = idx & 15;
  const int m = lane & 15, gq = lane >> 4;
  const int kh = w >> 2, wr = w & 3;
  u64* flg = (u64*)(ws + WS_FLG);
  const f32x4 z4 = {0, 0, 0, 0};
  const int rh = j >> 3, cb = j & 7;
  const int t0 = j < 6 ? j * 6 : 36 + (j - 6) * 5;
  const bool nf5 = j >= 6;
  const unsigned l16 = lane * 16;
  const char* wt = ws + WS_WT;
  const char* modp = ws + WS_MODP;
  bf16x8 pq[2];
  bf16x8 w2f[8][4];
  f32x4 gav[2], gbv[2], s2v[2];
  const unsigned fpos = (unsigned)((m + 16 * (gq >> 1)) * 16);
  const int n2 = cb * 64 + kh * 32 + gq * 4;
  u64 tag;
  {
    const int g = j, head = 2 * g + kh;
    u32x2 ev; unsigned c2; u32x4 wm[2], xr; unsigned wp0, wp1, wp2, wp3;
    const int bb = b < 192 ? b : b - 192, slab = bb >> 2, kq = bb & 3;
    const unsigned grow = (unsigned)(kq * 128 + w * 16 + (lane >> 3) * 2);
    GLD8S(ev, flg, 0u, 0);
    GLD4(c2, cvec, grow * 2, 0);
    {
      const char* wb = (const char*)(wmod + slab * 64);
      const unsigned wmo = grow * 6144 + (lane & 7) * 16;
      GLD16(wm[0], wb, wmo, 0); GLD16(wm[1], wb + 6144, wmo, 0);
    }
    GLD16(xr, (const char*)(x + (size_t)(r * 128 + j * 8 + w) * 512), l16, 0);
    {
      const char* wpb = (const char*)(wphi + head * 256);
      const unsigned o = (unsigned)(gq * 128 + m * 2);
      GLDU16(wp0, wpb, o, 0); GLDU16(wp1, wpb, o, 32); GLDU16(wp2, wpb, o, 64); GLDU16(wp3, wpb, o, 96);
    }
    __builtin_amdgcn_s_barrier();
    unsigned wu_;
    {
      const char* tb_ = w < 6 ? (const char*)wqkv + (idx * 6 + w) * 8192 : (const char*)wo + (idx * 2 + w - 6) * 8192;
      GLD4(wu_, tb_, (unsigned)(lane * 128), 0);
    }
    TS(27);
    WV4(1, c2, wm[0], wm[1], ev); WV4(1, wp0, wp1, wp2, wp3); WV1(1, xr);
    TS(28);
    tag = (((u64)__builtin_amdgcn_readfirstlane(ev[1]) << 32) | (u64)(unsigned)__builtin_amdgcn_readfirstlane(ev[0])) + 1ull;
    gemv_part(c2, wm, sm + L_SCR, w, lane);
    BAR();
    TS(1);
    if (w == 0) {
      float s = 0;
#pragma unroll
      for (int p = 0; p < 8; ++p) s += ((const float*)(sm + L_SCR))[p * 64 + lane];
      st8_sc1(mkrsrc(ws + WS_MODP, 98304), (unsigned)(kq * 24576 + (slab * 64 + lane) * 8), u32x2{F2U(s), (unsigned)tag});
    }
    const unsigned w1o = (unsigned)((lane >> 2) * 3072 + (((lane & 3) ^ (((lane >> 5) & 1) << 1)) * 16));
#define P1_W(c_, pc_) { const int pc__ = (pc_); const int khh_ = pc__ / 12, seg_ = (pc__ >> 2) % 3, rbk_ = pc__ & 3; \
      gblk((const char*)(wqkv + (size_t)(khh_ * 256 + (c_) * 64 + rbk_ * 16) * 1536 + seg_ * 512 + g * 32), w1o, w1_off(c_) + pc__ * 1024); }
#pragma unroll
    for (int i = 0; i < 9; ++i) P1_W(i / 3, w * 3 + i % 3);
    float xf[8], rs1, nb1;
    {
#pragma unroll
      for (int i = 0; i < 4; ++i) { xf[2 * i] = lo16(xr[i]); xf[2 * i + 1] = hi16(xr[i]); }
      float s = ((xf[0] + xf[1]) + (xf[2] + xf[3])) + ((xf[4] + xf[5]) + (xf[6] + xf[7]));
      s = xsum2(xsum1(s)); s += dpp<0x124>(s); s += dpp<0x128>(s); s = xsum32(xsum16(s));
      const float mean = s * (1.f / 512.f);
      float q = 0;
#pragma unroll
      for (int i = 0; i < 8; ++i) { const float d = xf[i] - mean; q = sfma(d, d, q); }
      q = xsum2(xsum1(q)); q += dpp<0x124>(q); q += dpp<0x128>(q); q = xsum32(xsum16(q));
      rs1 = 1.f / __builtin_sqrtf(q * (1.f / 512.f) + 1e-5f);
      nb1 = -mean * rs1;
    }
    TS(29);
    __builtin_amdgcn_s_barrier();
    {
      u32x4 mg[4];
      MODLD(mg, tid * 16);
      WV4(0, mg[0], mg[1], mg[2], mg[3]); WV1(0, wu_);
      const unsigned tl = (unsigned)tag;
      unsigned spins = 0;
      while (true) {
        bool ok = true;
#pragma unroll
        for (int k = 0; k < 4; ++k) ok = ok && mg[k][1] == tl && mg[k][3] == tl;
        if (__builtin_amdgcn_ballot_w64(!ok) == 0ull || ++spins > (1u << 18)) break;
        __builtin_amdgcn_s_sleep(1);
        MODLD(mg, tid * 16);
        WV4(0, mg[0], mg[1], mg[2], mg[3]);
      }
      TS(30);
      f32x2 mv = modsum(mg);
      if (tid >= 256) mv = rbf2(mv[0] + 1.f, mv[1] + 1.f);
      *(f32x2*)(sm + L_MOD1 + tid * 8) = mv;
      BAR();
      const char* a_ = sm + L_MOD1 + lane * 32;
      const f32x4 a0 = LDV4(a_), a1 = LDV4(a_ + 16), s0 = LDV4(a_ + 2048), s1 = LDV4(a_ + 2064);
      const bf16x8 hv = modulate2(xr, rs1, nb1, s0, s1, a0, a1);
      *(bf16x8*)(sm + L_HT + lane * 128 + ((w ^ (lane & 7)) * 16)) = hv;
      BAR();
      const int seg = tid >> 3, i8 = tid & 7;
      const u32x4 ho = *(const u32x4*)(sm + L_HT + seg * 128 + ((i8 ^ (seg & 7)) * 16));
      st16_sc1(mkrsrc(ws + WS_H, 2 << 20), (unsigned)(((r * 8 + (j >> 1)) * 16 + (seg >> 2)) * 1024 + ((j & 1) * 8 + i8 + 16 * (seg & 3)) * 16), ho);
    }
    TS(2);
    signal(flg + F_S1 + r * 16 + j, tag);
    TS(3);
    u32x4 tv[4]; int nvalid, rks; unsigned rdst, rstep, rup; bool risw3;
    const char* rsb; const char* rsb2; unsigned rso; int rpitch;
    {
      int u = w * 256 + b; const bool have = u < 1048; if (!have) u = b;
      const unsigned short* src; int nbk; nvalid = 4; risw3 = u < 704; rup = 0;
      if (risw3) { const int mat = u / 352, v2 = u % 352; rks = v2 / 22; nbk = v2 % 22; src = mat ? wu : wg; rpitch = 1376;
        rdst = WT_W3 + (unsigned)((mat * 86 + nbk * 4) * 16 + rks) * 1024; rstep = 16 * 1024; if (nbk == 21) nvalid = 2;
        rup = WT_UP + (unsigned)((mat * 16 + rks) * 1376 + nbk * 64) * 8; }
      else { const int v = u - 704; rks = v >> 3; nbk = v & 7; src = wd; rpitch = 512; rdst = WT_W4 + (unsigned)(nbk * 4 * 43 + rks) * 1024; rstep = 43 * 1024; }
      int ch = lane & 7; if (nvalid == 2) ch &= 3;
      if (!have) nvalid = 0;
      rsb = (const char*)(src + (size_t)(rks * 32) * rpitch + nbk * 64); rsb2 = rsb + (size_t)32 * rpitch;
      if (!risw3) { rsb = (const char*)(src + (size_t)(pos2ft(2 * rks) * 16) * 512 + nbk * 64); rsb2 = (const char*)(src + (size_t)(pos2ft(2 * rks + 1) * 16) * 512 + nbk * 64); }
      rso = (unsigned)((lane >> 3) * rpitch * 2 + ch * 16);
      if (!have) { rsb = modp; rsb2 = modp; rso = l16; rpitch = 0; }
    }
    if (w < 4) {
#pragma unroll
      for (int i = 0; i < 6; ++i) P1_W(3, w * 6 + i);
    }
    u32x4 m2[4], gv[4];
    const int gl = w == 0 ? lane : 0;
    const unsigned gvo = (unsigned)(((gl & 32) ? 2560 : 1024) + cb * 64 + (gl & 31) * 2) * 8;
    MODLD(m2, (unsigned)(1536 * 8 + tid * 16));
    MODLD(gv, gvo);
    waitflags(flg + F_S1 + r * 16, 16, tag, w ^ 7, lane);
    TS(4);
    f32x4 acc[3][2][2];
#pragma unroll
    for (int s = 0; s < 3; ++s)
#pragma unroll
      for (int hb = 0; hb < 2; ++hb)
#pragma unroll
        for (int mt = 0; mt < 2; ++mt) acc[s][hb][mt] = z4;
    {
      u32x4 ha[2][8];
      const char* hb0 = ws + WS_H + (size_t)((r * 8 + wr * 2) * 16 + kh * 8) * 1024;
#define P1_H(ks_) { const char* hb_ = hb0 + ((ks_) >> 2) * 4096; GLD16S(ha[0][ks_], hb_, l16, ((ks_) & 3) * 1024); GLD16S(ha[1][ks_], hb_ + 16384, l16, ((ks_) & 3) * 1024); }
#pragma unroll
      for (int ks = 0; ks < 4; ++ks) P1_H(ks);
      const int q = (lane & 15) >> 2, p = lane & 3;
      LB tb[2];
#pragma unroll
      for (int hb = 0; hb < 2; ++hb) tb[hb] = mklb(kh * 12288 + (gq * 8 + q) * 64 + ((((hb ^ kh) * 2 + (p >> 1)) ^ ((gq & 1) << 1)) * 16) + (p & 1) * 8);
      bf16x8 wf[2][6];
#define P1_LD(s_) _Pragma("unroll") for (int i = 0; i < 6; ++i) { constexpr int o_ = w1_off((s_) >> 1) + ((s_) & 1) * 2048; wf[(s_) & 1][i] = cat8(TRB(tb[i & 1], o_ + (i >> 1) * 4096), TRB(tb[i & 1], o_ + (i >> 1) * 4096 + 256)); }
      P1_LD(0);
      FENCE();
      TS(5);
      const unsigned w2o = (unsigned)((lane >> 3) * 1024 + (((lane & 7) ^ ((((lane >> 4) & 1) | ((w & 1) << 1)) << 1)) * 16));
#define P1_WO(i_) gblk((const char*)(wo + (size_t)(((i_) * 8 + w) * 8) * 512 + cb * 64), w2o, L_W2 + ((i_) * 8 + w) * 1024)
      static_for<0, 8>([&](auto S) __attribute__((always_inline)) NOPK {
        constexpr int s = decltype(S)::value;
        if constexpr (s == 5) BAR();
        if constexpr (s < 7) { P1_LD(s + 1); }
        if constexpr (s < 4) P1_H(s + 4);
        WV2(s < 4 ? 8 : 2 * (7 - s) + (s > 4 ? ((s - 4) >> 1) * 4 : 0), ha[0][s], ha[1][s]);
        FENCE();
#pragma unroll
        for (int i = 0; i < 6; ++i)
#pragma unroll
          for (int mt = 0; mt < 2; ++mt) {
            const int sg = i >> 1, hb = i & 1;
            if (sg < 2) acc[sg][hb][mt] = mfma32(wf[s & 1][i], __builtin_bit_cast(bf16x8, ha[mt][s]), acc[sg][hb][mt]);
            else acc[sg][hb][mt] = mfma32(__builtin_bit_cast(bf16x8, ha[mt][s]), wf[s & 1][i], acc[sg][hb][mt]);
          }
        FENCE();
        if constexpr (s >= 4) { if (kh == (s & 1)) { constexpr int q0 = ((s - 4) >> 1) * 4; P1_WO(q0); P1_WO(q0 + 1); P1_WO(q0 + 2); P1_WO(q0 + 3); } }
      });
    }
    BAR();
    TS(6);
#pragma unroll
    for (int s = 0; s < 3; ++s)
#pragma unroll
      for (int mt = 0; mt < 2; ++mt) *(f32x4*)(sm + (w * 6 + s * 2 + mt) * 1024 + l16) = acc[s][1][mt];
#pragma unroll
    for (int i = 0; i < 4; ++i) GLD16(tv[i], (i < 2 ? rsb : rsb2) + (size_t)(i & 1) * 16 * rpitch, rso, 0);
    BAR();
    f32x4 t[3][2];
#pragma unroll
    for (int s = 0; s < 3; ++s)
#pragma unroll
      for (int mt = 0; mt < 2; ++mt) {
        const f32x4 o = *(const f32x4*)(sm + ((w ^ 4) * 6 + s * 2 + mt) * 1024 + l16);
        t[s][mt] = acc[s][0][mt] + o;
      }
    TS(7);
    const bf16x4 wpT = __builtin_bit_cast(bf16x4, u32x2{wp0 | (wp1 << 16), wp2 | (wp3 << 16)});
    bf16x4 ident;
#pragma unroll
    for (int jj = 0; jj < 4; ++jj) ident[jj] = (4 * gq + jj == m) ? (short)0x3f80 : (short)0;
    bf16x4 pkp[2], pkn[2];
#pragma unroll
    for (int mt = 0; mt < 2; ++mt) {
      bf16x4 pos, neg;
      featmap(mfma16(wpT, pack4(t[1][mt]), z4), pos, neg);
      pkp[mt] = pack4(mfma16(pos, ident, z4));
      pkn[mt] = pack4(mfma16(neg, ident, z4));
    }
    const bf16x8 vf = cat8(pack4(t[2][0]), pack4(t[2][1]));
    const bf16x8 fp = cat8(pkp[0], pkp[1]), fn = cat8(pkn[0], pkn[1]);
    const bf16x8 ones = {0x3f80, 0x3f80, 0x3f80, 0x3f80, 0x3f80, 0x3f80, 0x3f80, 0x3f80};
    f32x4 part[4];
    part[0] = mfma32(fp, vf, z4); part[1] = mfma32(fn, vf, z4); part[2] = mfma32(fp, ones, z4); part[3] = mfma32(fn, ones, z4);
    BAR();
#pragma unroll
    for (int i = 0; i < 4; ++i) *(f32x4*)(sm + (w * 4 + i) * 1024 + l16) = part[i];
    BAR();
    TS(8);
    {
      const rsrc_t rkv = mkrsrc(ws + WS_KVP, 16 * 32 * KVP_STRIDE);
      f32x4 s = z4;
#pragma unroll
      for (int jj = 0; jj < 4; ++jj) s += *(const f32x4*)(sm + ((kh * 4 + jj) * 4 + wr) * 1024 + l16);
      const unsigned base = (unsigned)((r * 32 + head) * KVP_STRIDE);
      if (wr < 2) st16_sc1(rkv, base + wr * 1024 + l16, __builtin_bit_cast(u32x4, s));
      else if (m == 0) st16_sc1(rkv, base + 2048 + (wr - 2) * 64 + gq * 16, __builtin_bit_cast(u32x4, s));
    }
    signal(flg + F_S2 + g * 16 + r, tag);
    TS(9);
    {
      asm volatile("" : "+v"(tv[0]), "+v"(tv[1]), "+v"(tv[2]), "+v"(tv[3]));
      WV4(0, m2[0], m2[1], m2[2], m2[3]); WV4(0, gv[0], gv[1], gv[2], gv[3]);
      {
        const unsigned tl = (unsigned)tag;
        unsigned spins = 0;
        while (true) {
          bool ok = true;
#pragma unroll
          for (int k = 0; k < 4; ++k) ok = ok && m2[k][1] == tl && m2[k][3] == tl && gv[k][1] == tl && gv[k][3] == tl;
          if (__builtin_amdgcn_ballot_w64(!ok) == 0ull || ++spins > (1u << 18)) break;
          __builtin_amdgcn_s_sleep(1);
          MODLD(m2, (unsigned)(1536 * 8 + tid * 16)); MODLD(gv, gvo);
          WV4(0, m2[0], m2[1], m2[2], m2[3]); WV4(0, gv[0], gv[1], gv[2], gv[3]);
        }
      }
      f32x2 mv2 = modsum(m2);
      if (tid >= 256) mv2 = rbf2(mv2[0] + 1.f, mv2[1] + 1.f);
      *(f32x2*)(sm + L_M2V + tid * 8) = mv2;
      if (w == 0) *(f32x2*)(sm + L_GV + lane * 8) = modsum(gv);
      char* lw = sm + w * 4096;
#pragma unroll
      for (int i = 0; i < 4; ++i) {
        const int rl = i * 8 + (lane >> 3);
        const int sw = ((rl >> 1) & 1) | (((rl >> 3) & 1) << 1);
        *(u32x4*)(lw + rl * 128 + (((lane & 7) ^ (sw << 1)) * 16)) = tv[i];
      }
      BAR();
#pragma unroll
      for (int nt = 0; nt < 2; ++nt) { gav[nt] = LDV4(sm + L_GV + (kh * 32 + nt * 16 + gq * 4) * 4); gbv[nt] = LDV4(sm + L_GV + 256 + (kh * 32 + nt * 16 + gq * 4) * 4); s2v[nt] = LDV4(sm + L_M2V + 2048 + (n2 + nt * 16) * 4); }
      if (nvalid > 0) {
        const int q4 = (lane & 15) >> 2, p4 = lane & 3;
        const int sw = (q4 >> 1) | ((gq & 1) << 1);
        const rsrc_t rwt = mkrsrc(ws + WS_WT, 6 << 20);
        bf16x8 f[4];
#pragma unroll
        for (int nt = 0; nt < 4; ++nt) f[nt] = trfrag(lw + (gq * 8 + q4) * 128 + ((nt ^ sw) * 32) + p4 * 8, 512);
        if (risw3) {
          const char* a_ = sm + L_M2V + (rks * 32 + gq * 8) * 4 + ((m & 2) ? 0 : 2048);
          const f32x4 b0 = LDV4(a_), b1 = LDV4(a_ + 16);
          u32x4 hi = {pack2(b0[0], b0[1]), pack2(b0[2], b0[3]), pack2(b1[0], b1[1]), pack2(b1[2], b1[3])};
          u32x4 lo = {pack2(b0[0] - lo16(hi[0]), b0[1] - hi16(hi[0])), pack2(b0[2] - lo16(hi[1]), b0[3] - hi16(hi[1])),
                      pack2(b1[0] - lo16(hi[2]), b1[1] - hi16(hi[2])), pack2(b1[2] - lo16(hi[3]), b1[3] - hi16(hi[3]))};
          const u32x4 zz = {0, 0, 0, 0};
          const bf16x8 sbA = __builtin_bit_cast(bf16x8, m >= 4 ? zz : ((m & 1) ? lo : hi));
#pragma unroll
          for (int nt = 0; nt < 4; ++nt) {
            const f32x4 d = mfma32(sbA, f[nt], z4);
            if (nt < nvalid && gq == 0) st8_sc1(rwt, rup + (unsigned)(nt * 16 + m) * 8, u32x2{F2U(d[0] + d[1]), F2U(d[2] + d[3])});
          }
        }
#pragma unroll
        for (int nt = 0; nt < 4; ++nt) if (nt < nvalid) st16_sc1(rwt, rdst + nt * rstep + l16, __builtin_bit_cast(u32x4, f[nt]));
      }
      WAITV(0);
    }
    TS(10);
    waitflags(flg + F_S2 + g * 16, 16, tag, w ^ 7, lane);
    if (tid == 0) stflag(flg + F_RT + b, tag);
    TS(11);
    {
      u32x4 pl[4][2]; u32x2 zl;
      const char* kb = ws + WS_KVP + (size_t)((wr * 4) * 32 + head) * KVP_STRIDE;
#pragma unroll
      for (int i = 0; i < 4; ++i) {
        const char* kbi = kb + (size_t)i * 32 * KVP_STRIDE;
        GLD16S(pl[i][0], kbi, l16, 0); GLD16S(pl[i][1], kbi, l16, 1024);
      }
      GLD8S(zl, kb, (unsigned)(gq * 32 * KVP_STRIDE + m * 8), 2048);
#pragma unroll
      for (int mt = 0; mt < 2; ++mt) {
        bf16x4 pos, neg;
        featmap(mfma16(wpT, pack4(t[0][mt]), z4), pos, neg);
        pq[mt] = cat8(pos, neg);
      }
      asm volatile("" : "+v"(pq[0]), "+v"(pq[1]));
      FENCE();
      WV4(0, pl[0][0], pl[0][1], pl[1][0], pl[1][1]); WV4(0, pl[2][0], pl[2][1], pl[3][0], pl[3][1]); WV1(0, zl);
#pragma unroll
      for (int k = 0; k < 2; ++k)
        *(f32x4*)(sm + 65536 + (w * 2 + k) * 1024 + l16) =
            (__builtin_bit_cast(f32x4, pl[0][k]) + __builtin_bit_cast(f32x4, pl[1][k])) + (__builtin_bit_cast(f32x4, pl[2][k]) + __builtin_bit_cast(f32x4, pl[3][k]));
      const float z0 = xsum32(xsum16(U2F(zl[0]))), z1 = xsum32(xsum16(U2F(zl[1])));
      if (gq == 0) *(f32x2*)(sm + w * 128 + m * 8) = f32x2{z0, z1};
    }
    BAR();
    TS(12);
    f32x4 tt[4] = {z4, z4, z4, z4};
#pragma unroll
    for (int jj = 0; jj < 4; ++jj) {
      tt[0] += *(const f32x4*)(sm + 65536 + ((kh * 4 + jj) * 2 + 0) * 1024 + l16);
      tt[1] += *(const f32x4*)(sm + 65536 + ((kh * 4 + jj) * 2 + 1) * 1024 + l16);
      tt[2] += *(const f32x4*)(sm + ((kh * 4 + jj) * 2 + 0) * 64 + gq * 16);
      tt[3] += *(const f32x4*)(sm + ((kh * 4 + jj) * 2 + 1) * 64 + gq * 16);
      if (jj == 1) FENCE();
    }
    const bf16x8 kvA = cat8(pack4(tt[0]), pack4(tt[1])), zA = cat8(pack4(tt[2]), pack4(tt[3]));
    const rsrc_t ro = mkrsrc(ws + WS_O, 2 << 20);
    u32x4 rtf;
    GLD16S(rtf, flg + F_RT + w * 32, (unsigned)((lane & 15) * 16), 0);
    u32x2 op[2];
#pragma unroll
    for (int mt = 0; mt < 2; ++mt) {
      f32x4 num = mfma32(kvA, pq[mt], z4), den = mfma32(zA, pq[mt], z4);
      const float inv = 1.f / (den[0] + 1e-6f);
      num *= inv;
      op[mt] = __builtin_bit_cast(u32x2, pack4(num));
    }
    st16_sc1(ro, (unsigned)(((r * 8 + wr * 2 + (gq & 1)) * 16 + g) * 1024 + kh * 512) + fpos, widen(op[0], op[1]));
    TS(13);
    {
      WV1(0, rtf);
      const unsigned tl = (unsigned)tag, th = (unsigned)(tag >> 32);
      const bool ok = rtf[0] == tl && rtf[1] == th && rtf[2] == tl && rtf[3] == th;
      if (__builtin_amdgcn_ballot_w64(!ok) != 0ull) spinflags(flg + F_RT + w * 32, 32, tag, lane);
    }
    signal(flg + F_S3 + r * 16 + j, tag);
    TS(14);
  }
  u32x2 x1p[2];
  const int mtile2 = r * 8 + rh * 4 + wr, row2 = mtile2 * 16 + m;
  {
    u32x2 xv[2], yv[2]; float x1v[8];
    float u1 = 0, u2 = 0;
    if (w >= 4) {
#pragma unroll
      for (int i = 0; i < 12; ++i) w3_piece(wt, l16, t0, nf5, i / 6, (w - 4) * 6 + i % 6);
    } else if (w >= 1) {
      const int t = tid - 64, mat = t / 96; int cc = t % 96; if (nf5 && cc >= 80) cc -= 16;
      const unsigned uo = (unsigned)((mat * 16 * 1376 + t0 * 16 + cc) * 8);
      u32x2 uv[16];
#pragma unroll
      for (int k = 0; k < 16; ++k) GLD8S(uv[k], wt + WT_UP + k * 11008, uo, 0);
      WV4(0, uv[0], uv[1], uv[2], uv[3]); WV4(0, uv[4], uv[5], uv[6], uv[7]); WV4(0, uv[8], uv[9], uv[10], uv[11]); WV4(0, uv[12], uv[13], uv[14], uv[15]);
#pragma unroll
      for (int k = 0; k < 16; ++k) { u1 += U2F(uv[k][0]); u2 += U2F(uv[k][1]); }
    }
      {
        const int q4 = (lane & 15) >> 2, p4 = lane & 3;
        const int sw = (q4 >> 1) | ((gq & 1) << 1);
#pragma unroll
        for (int nt = 0; nt < 4; ++nt) {
          const char* fb = sm + L_W2 + kh * 32768 + (gq * 8 + q4) * 128 + ((nt ^ (kh * 2) ^ sw) * 32) + p4 * 8;
#pragma unroll
          for (int s = 0; s < 8; ++s) w2f[s][nt] = trfrag(fb + s * 4096, 512);
        }
      }
    waitflags(flg + F_S3 + r * 16, 16, tag, w, lane);
    TS(15);
    u32x4 af[8];
    const char* ob = ws + WS_O + (size_t)(mtile2 * 16 + kh * 8) * 1024;
#pragma unroll
    for (int ks = 0; ks < 4; ++ks) GLD16S(af[ks], ob, l16, ks * 1024);
#pragma unroll
    for (int ks = 4; ks < 8; ++ks) GLD16S(af[ks], ob + 4096, l16, (ks - 4) * 1024);
    GLD8(xv[0], x, (unsigned)(row2 * 1024 + n2 * 2), 0); GLD8(xv[1], x, (unsigned)(row2 * 1024 + n2 * 2), 32);
    f32x4 acc[4] = {z4, z4, z4, z4};
#pragma unroll
    for (int ks = 0; ks < 8; ++ks) {
      WV1(7 - ks + 2, af[ks]);
      FENCE();
#pragma unroll
      for (int nt = 0; nt < 4; ++nt) acc[nt] = mfma32(w2f[ks][nt], __builtin_bit_cast(bf16x8, af[ks]), acc[nt]);
      FENCE();
    }
#pragma unroll
    for (int nt = 0; nt < 2; ++nt) *(f32x4*)(sm + L_TOP + (w * 2 + nt) * 1024 + l16) = acc[2 + nt];
    WV2(0, xv[0], xv[1]);
#pragma unroll
    for (int i = 0; i < 3; ++i) w3_piece(wt, l16, t0, nf5, 2, w * 3 + i);
    BAR();
    const rsrc_t rx1 = mkrsrc(ws + WS_X1, 2 << 20), rst = mkrsrc(ws + WS_ST2, 256 << 10);
#pragma unroll
    for (int nt = 0; nt < 2; ++nt) {
      const f32x4 a = acc[nt] + *(const f32x4*)(sm + L_TOP + ((w ^ 4) * 2 + nt) * 1024 + l16);
      const f32x4 gaf = gav[nt];
      x1v[nt * 4 + 0] = lo16(xv[nt][0]) + gaf[0] * a[0]; x1v[nt * 4 + 1] = hi16(xv[nt][0]) + gaf[1] * a[1];
      x1v[nt * 4 + 2] = lo16(xv[nt][1]) + gaf[2] * a[2]; x1v[nt * 4 + 3] = hi16(xv[nt][1]) + gaf[3] * a[3];
      x1p[nt] = u32x2{pack2(x1v[nt * 4], x1v[nt * 4 + 1]), pack2(x1v[nt * 4 + 2], x1v[nt * 4 + 3])};
      yv[nt] = u32x2{pack2(x1v[nt * 4] * s2v[nt][0], x1v[nt * 4 + 1] * s2v[nt][1]), pack2(x1v[nt * 4 + 2] * s2v[nt][2], x1v[nt * 4 + 3] * s2v[nt][3])};
    }
    st16_sc1(rx1, (unsigned)((mtile2 * 16 + cb * 2 + kh) * 1024 + (gq & 1) * 512) + fpos, widen(yv[0], yv[1]));
    float s = ((x1v[0] + x1v[1]) + (x1v[2] + x1v[3])) + ((x1v[4] + x1v[5]) + (x1v[6] + x1v[7]));
    s = xsum32(xsum16(s));
    const float mean = s * (1.f / 32.f);
    float q2 = 0;
#pragma unroll
    for (int jj = 0; jj < 8; ++jj) { float d = x1v[jj] - mean; q2 += d * d; }
    q2 = xsum32(xsum16(q2));
    if (gq == 0) { f32x2 st = {mean, q2}; st8_sc1(rst, (unsigned)(row2 * 128 + (cb * 2 + kh) * 8), __builtin_bit_cast(u32x2, st)); }
    TS(16);
    signal(flg + F_S4 + r * 16 + j, tag);
    if (w >= 4) {
#pragma unroll
      for (int i = 0; i < 12; ++i) w3_piece(wt, l16, t0, nf5, 3 + i / 6, (w - 4) * 6 + i % 6);
    } else if (w >= 1) {
      const int t = tid - 64;
      ((float*)(sm + L_TOP))[t] = u1; ((float*)(sm + L_TOP + 768))[t] = u2;
    }
    waitflags(flg + F_S4 + r * 16, 16, tag, w, lane);
    TS(17);
  }
  if (j < 6) phase3<6>(sm, ws, r, j, t0, cb, w, lane, tag TSPASS);
  else phase3<5>(sm, ws, r, j, t0, cb, w, lane, tag TSPASS);
  {
    if (w >= 4) {
#pragma unroll
      for (int i = 12; i < 20; ++i) { w4_piece(wt, l16, cb, i, (w - 4) * 2); w4_piece(wt, l16, cb, i, (w - 4) * 2 + 1); }
    }
    const LB fb = mklb(kh * 4096 + l16);
    bf16x8 wf[4][4];
#pragma unroll
    for (int s = 0; s < 3; ++s)
#pragma unroll
      for (int nt = 0; nt < 4; ++nt) wf[s][nt] = LDB(fb, w4_off(s) + nt * 1024);
    waitflags(flg + F_S5 + r * 16, 16, tag, w, lane);
    TS(23);
    u32x4 af[22];
    const char* hb0 = ws + WS_HH + (size_t)(mtile2 * 43 + kh * 12) * 1024;
    const char* hb1 = ws + WS_HH + (size_t)(mtile2 * 43 + 24 + kh * 10) * 1024;
    const unsigned l21 = l16 + (kh ? 0u : 1024u);
#define P4_A(i_) { if ((i_) < 12) { GLD16S(af[i_], hb0 + ((i_) >> 2) * 4096, l16, ((i_) & 3) * 1024); } else if ((i_) < 21) { GLD16S(af[i_], hb1 + (((i_) - 12) >> 2) * 4096, l16, (((i_) - 12) & 3) * 1024); } else { GLD16S(af[21], hb1 + 8 * 1024, l21, 0); } }
    w4_piece(wt, l16, cb, 20, w); w4_piece(wt, l16, cb, 21, w);
#pragma unroll
    for (int i = 0; i < 8; ++i) P4_A(i);
    f32x4 acc[4] = {z4, z4, z4, z4};
#pragma unroll
    for (int i = 0; i < 22; ++i) {
      if (i == 6) {
        waitflags(flg + F_S5B + r * 16, 16, tag, w, lane);
#pragma unroll
        for (int k = 12; k < 18; ++k) P4_A(k);
      }
      if (i + 3 < 22) {
#pragma unroll
        for (int nt = 0; nt < 4; ++nt) wf[(i + 3) & 3][nt] = LDB(fb, w4_off(i + 3) + nt * 1024);
      }
      if (i < 4) P4_A(i + 8);
      if (i >= 8 && i < 12) P4_A(i + 10);
      WV1(i < 4 ? 8 : i < 6 ? 11 - i : i < 8 ? 17 - i : i < 12 ? 10 : 21 - i, af[i]);
      FENCE();
      if (i < 21 || kh == 0) {
#pragma unroll
        for (int nt = 0; nt < 4; ++nt) acc[nt] = mfma32(wf[i & 3][nt], __builtin_bit_cast(bf16x8, af[i]), acc[nt]);
      }
      FENCE();
    }
    TS(25);
    BAR();
#pragma unroll
    for (int nt = 0; nt < 2; ++nt) *(f32x4*)(sm + (w * 2 + nt) * 1024 + l16) = acc[2 + nt];
    BAR();
    u32x2 ov[2];
#pragma unroll
    for (int nt = 0; nt < 2; ++nt) {
      const f32x4 a = acc[nt] + *(const f32x4*)(sm + ((w ^ 4) * 2 + nt) * 1024 + l16);
      const f32x4 gbf = gbv[nt];
      ov[nt] = u32x2{pack2(lo16(x1p[nt][0]) + gbf[0] * a[0], hi16(x1p[nt][0]) + gbf[1] * a[1]), pack2(lo16(x1p[nt][1]) + gbf[2] * a[2], hi16(x1p[nt][1]) + gbf[3] * a[3])};
    }
    *(u32x4*)(out + (size_t)row2 * 512 + cb * 64 + kh * 32 + (gq & 1) * 16 + (gq >> 1) * 8) = widen(ov[0], ov[1]);
    if (b == 0 && tid == 0) stflag(flg + F_E, tag);
    TS(26);
    STAMP_FLUSH(1, ws);
  }
}

extern "C" void kernel_launch(void* const* d_in, const int* in_sizes, int n_in, void* d_out, int out_size, void* d_ws, size_t ws_size,
                              hipStream_t stream) {
  const unsigned short* x = (const unsigned short*)d_in[0];
  const unsigned short* wqkv = (const unsigned short*)d_in[1];
  const unsigned short* wphi = (const unsigned short*)d_in[2];
  const unsigned short* wo = (const unsigned short*)d_in[3];
  const unsigned short* wg = (const unsigned short*)d_in[5];
  const unsigned short* wu = (const unsigned short*)d_in[6];
  const unsigned short* wd = (const unsigned short*)d_in[7];
  const unsigned short* c = (const unsigned short*)d_in[8];
  const unsigned short* wm = (const unsigned short*)d_in[9];
  char* ws = (char*)d_ws;
  (void)hipFuncSetAttribute((const void*)k1_main, hipFuncAttributeMaxDynamicSharedMemorySize, 163840);
  k1_main<<<256, 512, 163840, stream>>>(x, wqkv, wphi, wo, wg, wu, wd, c, wm, ws, (unsigned short*)d_out);
}
```

```cpp
#include <hip/hip_runtime.h>
#include <hip/hip_bf16.h>
#include <stdint.h>

typedef __attribute__((ext_vector_type(8))) short bf16x8;
typedef __attribute__((ext_vector_type(4))) short bf16x4;
typedef __attribute__((ext_vector_type(4))) float f32x4;
typedef __attribute__((ext_vector_type(2))) float f32x2;
typedef __attribute__((ext_vector_type(2))) __bf16 bf2_t;
typedef __attribute__((ext_vector_type(4))) unsigned u32x4;
typedef __attribute__((ext_vector_type(2))) unsigned u32x2;
#define LAS __attribute__((address_space(3)))
#define U2F(x) __builtin_bit_cast(float, (unsigned)(x))
#define F2U(x) __builtin_bit_cast(unsigned, (float)(x))
#if defined(__HIP_DEVICE_COMPILE__) && __HIP_DEVICE_COMPILE__
#define NOPK __attribute__((target("no-packed-fp32-ops")))
#else
#define NOPK
#endif
#define DEVI __device__ __forceinline__ NOPK

DEVI unsigned pack2(float a, float b) { f32x2 p = {a, b}; bf2_t q = __builtin_convertvector(p, bf2_t); return __builtin_bit_cast(unsigned, q); }
DEVI float lo16(unsigned u) { return U2F(u << 16); }
DEVI float hi16(unsigned u) { return U2F(u & 0xffff0000u); }
DEVI float vmax(float a, float b) { return __builtin_amdgcn_fmed3f(a, b, __builtin_inff()); }
DEVI float vmin(float a, float b) { return __builtin_amdgcn_fmed3f(a, b, -__builtin_inff()); }
DEVI float vmax3(float a, float b, float c) { return vmax(vmax(a, b), c); }
DEVI float vmin3(float a, float b, float c) { return vmin(vmin(a, b), c); }
DEVI float xmax16(float v) { auto r = __builtin_amdgcn_permlane16_swap(F2U(v), F2U(v), false, false); return vmax(U2F(r[0]), U2F(r[1])); }
DEVI float xmax32(float v) { auto r = __builtin_amdgcn_permlane32_swap(F2U(v), F2U(v), false, false); return vmax(U2F(r[0]), U2F(r[1])); }
DEVI float xmin16(float v) { auto r = __builtin_amdgcn_permlane16_swap(F2U(v), F2U(v), false, false); return vmin(U2F(r[0]), U2F(r[1])); }
DEVI float xmin32(float v) { auto r = __builtin_amdgcn_permlane32_swap(F2U(v), F2U(v), false, false); return vmin(U2F(r[0]), U2F(r[1])); }
DEVI float xsum16(float v) { auto r = __builtin_amdgcn_permlane16_swap(F2U(v), F2U(v), false, false); return U2F(r[0]) + U2F(r[1]); }
DEVI float xsum32(float v) { auto r = __builtin_amdgcn_permlane32_swap(F2U(v), F2U(v), false, false); return U2F(r[0]) + U2F(r[1]); }
template <int CTRL> DEVI float dpp(float v) { return U2F(__builtin_amdgcn_update_dpp(0u, F2U(v), CTRL, 0xf, 0xf, true)); }
DEVI float xsum1(float v) { return v + dpp<0xB1>(v); }
DEVI float xsum2(float v) { return v + dpp<0x4E>(v); }
DEVI float fexp2(float x) { return __builtin_amdgcn_exp2f(x); }
DEVI float frcp(float x) { return __builtin_amdgcn_rcpf(x); }
DEVI f32x4 mfma32(bf16x8 a, bf16x8 b, f32x4 c) { return __builtin_amdgcn_mfma_f32_16x16x32_bf16(a, b, c, 0, 0, 0); }
DEVI f32x4 mfma16(bf16x4 a, bf16x4 b, f32x4 c) { return __builtin_amdgcn_mfma_f32_16x16x16bf16_1k(a, b, c, 0, 0, 0); }
DEVI bf16x4 pack4(f32x4 v) { u32x2 u = {pack2(v[0], v[1]), pack2(v[2], v[3])}; return __builtin_bit_cast(bf16x4, u); }
DEVI bf16x8 cat8(bf16x4 a, bf16x4 b) { return __builtin_shufflevector(a, b, 0, 1, 2, 3, 4, 5, 6, 7); }
DEVI bf16x4 trread(const char* lds) { return __builtin_amdgcn_ds_read_tr16_b64_v4i16((LAS bf16x4*)lds); }
DEVI bf16x8 trfrag(const char* lds, int step) { return cat8(trread(lds), trread(lds + step)); }
#define WAITV(n) asm volatile("s_waitcnt vmcnt(" #n ")" ::: "memory")
#define BAR() do { asm volatile("s_waitcnt lgkmcnt(0)" ::: "memory"); __builtin_amdgcn_s_barrier(); asm volatile("" ::: "memory"); } while (0)
#define FENCE() __builtin_amdgcn_sched_barrier(0)
template <int I> struct IC { static constexpr int value = I; };
template <int I, int N, class F> DEVI void static_for(F&& f) { if constexpr (I < N) { f(IC<I>{}); static_for<I + 1, N>(f); } }
typedef unsigned long long u64;
constexpr size_t WS_FLG = 0;
constexpr int F_E = 0, F_S1 = 16, F_S2 = 16 * 17, F_S3 = 16 * 33, F_S4 = 16 * 49, F_S5 = 16 * 65, F_RT = 16 * 81, F_S5B = 16 * 97;
constexpr size_t WS_MODP = 64 << 10;
constexpr size_t WS_H = 1 << 20;
constexpr size_t WS_KVP = 4 << 20;
constexpr size_t WS_O = 8 << 20;
constexpr size_t WS_X1 = 12 << 20;
constexpr size_t WS_ST2 = 16 << 20;
constexpr size_t WS_HH = 20 << 20;
constexpr size_t WS_WT = 32 << 20;
constexpr unsigned WT_UP = 0;
constexpr unsigned WT_W3 = 1 << 20;
constexpr unsigned WT_W4 = 4 << 20;
constexpr int KVP_STRIDE = 2304;

#define GLD16(dst, sbase, voff, imm) asm volatile("global_load_dwordx4 %0, %1, %2 offset:%3" : "=v"(dst) : "v"(voff), "s"(sbase), "i"(imm) : "memory")
#define GLD16S(dst, sbase, voff, imm) asm volatile("global_load_dwordx4 %0, %1, %2 offset:%3 sc1" : "=v"(dst) : "v"(voff), "s"(sbase), "i"(imm) : "memory")
#define GLD8(dst, sbase, voff, imm) asm volatile("global_load_dwordx2 %0, %1, %2 offset:%3" : "=v"(dst) : "v"(voff), "s"(sbase), "i"(imm) : "memory")
#define GLD8S(dst, sbase, voff, imm) asm volatile("global_load_dwordx2 %0, %1, %2 offset:%3 sc1" : "=v"(dst) : "v"(voff), "s"(sbase), "i"(imm) : "memory")
#define GLDU16(dst, sbase, voff, imm) asm volatile("global_load_ushort %0, %1, %2 offset:%3" : "=v"(dst) : "v"(voff), "s"(sbase), "i"(imm) : "memory")
#define WV1(n, a) asm volatile("s_waitcnt vmcnt(%1)" : "+v"(a) : "i"(n) : "memory")
#define WV2(n, a, b) asm volatile("s_waitcnt vmcnt(%2)" : "+v"(a), "+v"(b) : "i"(n) : "memory")
#define WV4(n, a, b, c, d) asm volatile("s_waitcnt vmcnt(%4)" : "+v"(a), "+v"(b), "+v"(c), "+v"(d) : "i"(n) : "memory")
#define WVN(n) asm volatile("s_waitcnt vmcnt(%0)" :: "i"(n) : "memory")
DEVI void gblk(const void* sbase, unsigned voff, unsigned l) {
  asm volatile("s_mov_b32 m0, %2\n\ts_nop 0\n\tglobal_load_lds_dwordx4 %0, %1" :: "v"(voff), "s"(sbase), "s"(l) : "memory");
}
DEVI void gblks(const void* sbase, unsigned voff, unsigned l) {
  asm volatile("s_mov_b32 m0, %2\n\ts_nop 0\n\tglobal_load_lds_dwordx4 %0, %1 sc1" :: "v"(voff), "s"(sbase), "s"(l) : "memory");
}

DEVI f32x2 rbf2(float a, float b) { const unsigned u = pack2(a, b); return f32x2{lo16(u), hi16(u)}; }
DEVI float rbf1(float a) { return lo16(pack2(a, a)); }
DEVI f32x4 rbf4(f32x4 v) { const f32x2 a = rbf2(v[0], v[1]), b = rbf2(v[2], v[3]); return f32x4{a[0], a[1], b[0], b[1]}; }
DEVI void featmap(f32x4 p, bf16x4& pos, bf16x4& neg) {
  constexpr float L2E = 1.4426950408889634f;
  p = rbf4(p);
  float mx = vmax(vmax(p[0], p[1]), vmax(p[2], p[3]));
  float mn = vmin(vmin(p[0], p[1]), vmin(p[2], p[3]));
  mx = xmax32(xmax16(mx)); mn = xmin32(xmin16(mn));
  f32x4 ep, en;
#pragma unroll
  for (int j = 0; j < 4; ++j) { ep[j] = fexp2((p[j] - mx) * L2E); en[j] = fexp2((mn - p[j]) * L2E); }
  float sp = (ep[0] + ep[1]) + (ep[2] + ep[3]), sn = (en[0] + en[1]) + (en[2] + en[3]);
  sp = xsum32(xsum16(sp)); sn = xsum32(xsum16(sn));
  const float ip = frcp(sp), in = frcp(sn);
  ep *= ip; en *= in;
  pos = pack4(ep); neg = pack4(en);
}

typedef __amdgpu_buffer_rsrc_t rsrc_t;
DEVI rsrc_t mkrsrc(const void* p, unsigned bytes) { return __builtin_amdgcn_make_buffer_rsrc((void*)p, 0, bytes, 0x00020000); }
DEVI void st16_sc1(rsrc_t r, unsigned off, u32x4 v) { __builtin_amdgcn_raw_buffer_store_b128(v, r, off, 0, 16); }
DEVI void st8_sc1(rsrc_t r, unsigned off, u32x2 v) { __builtin_amdgcn_raw_buffer_store_b64(v, r, off, 0, 16); }
DEVI void st4_sc1(rsrc_t r, unsigned off, unsigned v) { __builtin_amdgcn_raw_buffer_store_b32(v, r, off, 0, 16); }
DEVI u64 ldflag(const u64* p) { return __hip_atomic_load(p, __ATOMIC_RELAXED, __HIP_MEMORY_SCOPE_AGENT); }
DEVI void stflag(u64* p, u64 v) { __hip_atomic_store(p, v, __ATOMIC_RELAXED, __HIP_MEMORY_SCOPE_AGENT); }

DEVI void signal(u64* f, u64 tag) {
  WAITV(0);
  BAR();
  if (threadIdx.x == 0) stflag(f, tag);
}
DEVI void spinflags(const u64* f, int n, u64 tag, int lane) {
  const u64* p = f + (lane < n ? lane : 0);
  unsigned spins = 0;
  while (__builtin_amdgcn_ballot_w64(ldflag(p) != tag) != 0ull) {
    __builtin_amdgcn_s_sleep(1);
    if (++spins > (1u << 20)) break;
  }
}
DEVI void waitflags(const u64* f, int n, u64 tag, int w, int lane) {
  if (w == 0) spinflags(f, n, tag, lane);
  BAR();
}

constexpr int SLOT = 24576, L_TOP = 147456;
constexpr int L_W2 = 98304;
constexpr int L_SCR = 147456, L_MOD1 = 149504, L_HT = 153600;
constexpr int L_M2V = 49152, L_GV = 53248;
DEVI constexpr int w1_off(int c) { return c * SLOT; }
DEVI constexpr int w3_slot(int ks) { return (ks % 6) * SLOT; }
DEVI constexpr int w4_off(int i) { return i < 12 ? 2 * SLOT + i * 8192 : i < 18 ? (i - 12) * 8192 : i < 20 ? L_TOP + (i - 18) * 8192 : 2 * SLOT + (i - 20) * 8192; }
DEVI void w3_q(const char* w3q, unsigned lq, unsigned l16, bool nf5, int st, int f3) {
  const int f = (st >> 2) * 3 + f3;
  const unsigned o = (unsigned)(((f == 5 && nf5) ? 4 : f) * 16 + (st & 3) * 2) * 1024;
  gblks(w3q + o, l16, lq + w3_slot(st) + f3 * 1024);
}
DEVI const char* w3_base(const char* wt, int t0, int q3) { return wt + WT_W3 + (size_t)(((q3 & 1) * 86 + t0) * 16 + ((q3 >> 1) & 1) * 8 + (q3 >> 2)) * 1024; }
DEVI void w4_w(const char* w4w, unsigned lw, unsigned l16, int khh, int i) {
  const int d = i < 12 ? i : (i < 21 ? 12 + i - 2 * khh : 33 - 3 * khh);
  gblks(w4w + d * 1024, l16, lw + w4_off(i));
}
DEVI const char* w4_base(const char* wt, int cb, int pc) { const int khh = pc >> 2, nt = (pc & 3) ^ (khh * 2); return wt + WT_W4 + (size_t)((cb * 4 + nt) * 43 + khh * 12) * 1024; }
DEVI int pos2ft(int p) {
  int jj, f;
  if (p < 48) { jj = p / 3; f = p % 3; } else { const int q = p - 48; if (q < 18) { jj = q / 3; f = 3 + q % 3; } else { jj = 6 + (q - 18) / 2; f = 3 + (q - 18) % 2; } }
  return (jj < 6 ? jj * 6 : 36 + (jj - 6) * 5) + f;
}

DEVI float sfma(float a, float b, float c) { return __builtin_fmaf(a, b, c); }
DEVI bf16x8 modulate2(u32x4 xv, float a, float c, f32x4 s0, f32x4 s1, f32x4 a0, f32x4 a1) {
  u32x4 o;
  o[0] = pack2(sfma(sfma(lo16(xv[0]), a, c), s0[0], a0[0]), sfma(sfma(hi16(xv[0]), a, c), s0[1], a0[1]));
  o[1] = pack2(sfma(sfma(lo16(xv[1]), a, c), s0[2], a0[2]), sfma(sfma(hi16(xv[1]), a, c), s0[3], a0[3]));
  o[2] = pack2(sfma(sfma(lo16(xv[2]), a, c), s1[0], a1[0]), sfma(sfma(hi16(xv[2]), a, c), s1[1], a1[1]));
  o[3] = pack2(sfma(sfma(lo16(xv[3]), a, c), s1[2], a1[2]), sfma(sfma(hi16(xv[3]), a, c), s1[3], a1[3]));
  return __builtin_bit_cast(bf16x8, o);
}
DEVI float dot2(unsigned a, unsigned b, float c) { return __builtin_amdgcn_fdot2_f32_bf16(__builtin_bit_cast(bf2_t, a), __builtin_bit_cast(bf2_t, b), c, false); }
#define LDF(p) (*(const bf16x8*)(p))
#define LDV4(p) (*(const f32x4*)(p))
struct LB { const LAS char* b[3]; };
DEVI LB mklb(unsigned off) { LB l; unsigned a0 = off, a1 = off + 65536, a2 = off + 131072; asm("" : "+v"(a0)); asm("" : "+v"(a1)); asm("" : "+v"(a2));
  l.b[0] = (const LAS char*)(size_t)a0; l.b[1] = (const LAS char*)(size_t)a1; l.b[2] = (const LAS char*)(size_t)a2; return l; }
#define LDB(lb, off) (*(const LAS bf16x8*)((lb).b[(off) >> 16] + ((off) & 65535)))
#define TRB(lb, off) __builtin_amdgcn_ds_read_tr16_b64_v4i16((LAS bf16x4*)((lb).b[(off) >> 16] + ((off) & 65535)))
#define MIX(n, k) _Pragma("unroll") for (int i_ = 0; i_ < (n); ++i_) { __builtin_amdgcn_sched_group_barrier(0x008, 1, 0); __builtin_amdgcn_sched_group_barrier(0x002, (k), 0); }

DEVI void gemv_part(const unsigned c2, const u32x4 (&wm)[2], char* scr, int w, int lane) {
  float acc[8];
  const float c0 = lo16(c2), c1 = hi16(c2);
  const f32x2 sr = rbf2(c0 / (1.f + fexp2(c0 * -1.4426950408889634f)), c1 / (1.f + fexp2(c1 * -1.4426950408889634f)));
  const float s0 = sr[0], s1 = sr[1];
#pragma unroll
  for (int j = 0; j < 4; ++j) { acc[2 * j] = sfma(s1, lo16(wm[1][j]), s0 * lo16(wm[0][j])); acc[2 * j + 1] = sfma(s1, hi16(wm[1][j]), s0 * hi16(wm[0][j])); }
#pragma unroll
  for (int j = 0; j < 8; ++j) { float v = acc[j]; v += dpp<0x128>(v); acc[j] = xsum32(xsum16(v)); }
  if (lane < 8) {
    float* d = (float*)scr + w * 64 + lane * 8;
    *(f32x4*)d = f32x4{acc[0], acc[1], acc[2], acc[3]}; *(f32x4*)(d + 4) = f32x4{acc[4], acc[5], acc[6], acc[7]};
  }
}
DEVI f32x2 modsum(const u32x4 (&g)[4]) {
  return rbf2((U2F(g[0][0]) + U2F(g[1][0])) + (U2F(g[2][0]) + U2F(g[3][0])), (U2F(g[0][2]) + U2F(g[1][2])) + (U2F(g[2][2]) + U2F(g[3][2])));
}
#define GLD4(dst, sbase, voff, imm) asm volatile("global_load_dword %0, %1, %2 offset:%3" : "=v"(dst) : "v"(voff), "s"(sbase), "i"(imm) : "memory")
#define MODLD(g, voff) { GLD16S(g[0], modp, voff, 0); GLD16S(g[1], modp + 24576, voff, 0); GLD16S(g[2], modp + 49152, voff, 0); GLD16S(g[3], modp + 73728, voff, 0); }

DEVI u32x4 widen(u32x2 a, u32x2 b) {
  auto r0 = __builtin_amdgcn_permlane16_swap(a[0], b[0], false, false);
  auto r1 = __builtin_amdgcn_permlane16_swap(a[1], b[1], false, false);
  return u32x4{r0[0], r1[0], r0[1], r1[1]};
}

template <int NF>
DEVI void phase3(char* sm, char* ws, int r, int j, int t0, int cb, int w, int lane, u64 tag) {
  u64* flg = (u64*)(ws + WS_FLG);
  const int wr = w & 3, kh = w >> 2, m = lane & 15, gq = lane >> 4;
  const unsigned l16 = lane * 16;
  const char* wt = ws + WS_WT;
  const rsrc_t rhh = mkrsrc(ws + WS_HH, 128 * 43 * 1024);
  const int mtile = r * 8 + wr * 2 + kh;
  u32x4 xa[2][8], sta[2];
  const char* xb0 = ws + WS_X1 + (size_t)(mtile * 16 + kh * 8) * 1024;
  const char* xb1 = ws + WS_X1 + (size_t)((mtile ^ 1) * 16 + kh * 8) * 1024;
#define P3_X(ks_) { GLD16S(xa[0][ks_], xb0 + ((ks_) >> 2) * 4096, l16, ((ks_) & 3) * 1024); GLD16S(xa[1][ks_], xb1 + ((ks_) >> 2) * 4096, l16, ((ks_) & 3) * 1024); }
  P3_X(0); P3_X(1); P3_X(2); P3_X(3);
  const char* w3w = w3_base(wt, t0, w); const unsigned l3w = (unsigned)w * 3072;
  const char* w4w = w4_base(wt, cb, w); const unsigned l4w = (unsigned)w * 1024;
  const char* stb = ws + WS_ST2 + (size_t)(mtile * 16) * 128;
  u32x4& sta0 = sta[0]; u32x4& sta1 = sta[1];
  const LB fb = mklb(kh * 6144 + l16);
  bf16x8 wf[3][3];
  f32x4 acc[2][3][2];
  float rs = 0, nb = 0;
#define P3_NT(gi_) (((gi_) >= 16 && NF == 5) ? 2 : 3)
#define P3_LD(gi_) _Pragma("unroll") for (int f3 = 0; f3 < P3_NT(gi_); ++f3) wf[(gi_) % 3][f3] = LDB(fb, w3_slot((gi_) >> 2) + (((gi_) >> 1) & 1) * 12288 + ((gi_) & 1) * 3072 + f3 * 1024);
#define P3_MM(gi_) _Pragma("unroll") for (int f3 = 0; f3 < P3_NT(gi_); ++f3) { constexpr int ks_ = ((gi_) >> 1) & 7; constexpr bool first_ = ks_ == 0; \
    acc[(gi_) & 1][f3][0] = mfma32(wf[(gi_) % 3][f3], __builtin_bit_cast(bf16x8, xa[0][ks_]), first_ ? f32x4{0, 0, 0, 0} : acc[(gi_) & 1][f3][0]); \
    acc[(gi_) & 1][f3][1] = mfma32(wf[(gi_) % 3][f3], __builtin_bit_cast(bf16x8, xa[1][ks_]), first_ ? f32x4{0, 0, 0, 0} : acc[(gi_) & 1][f3][1]); }
  const char* ub = sm + L_TOP + gq * 16;
#define P3_EPI(half_, xoff_) { \
    constexpr int nt_ = ((half_) && NF == 5) ? 2 : 3; \
    char* xw = sm + (xoff_) + w * 6144 + l16; const char* xr = sm + (xoff_) + (w ^ 4) * 6144 + l16; \
    _Pragma("unroll") for (int a = 0; a < 2; ++a) _Pragma("unroll") for (int f3 = 0; f3 < nt_; ++f3) *(f32x4*)(xw + (a * 3 + f3) * 1024) = acc[a][f3][1]; \
    BAR(); \
    u32x2 hp[3]; \
    const int p0 = (half_) ? 48 + (j < 6 ? 3 * j : 18 + 2 * (j - 6)) : 3 * j; \
    _Pragma("unroll") for (int f3 = 0; f3 < nt_; ++f3) { \
      const int f = (half_) * 3 + f3; \
      const f32x4 gv = acc[0][f3][0] + *(const f32x4*)(xr + f3 * 1024), uv = acc[1][f3][0] + *(const f32x4*)(xr + (3 + f3) * 1024); \
      const f32x4 g1 = LDV4(ub + f * 64), g2 = LDV4(ub + 768 + f * 64), u1 = LDV4(ub + 384 + f * 64), u2 = LDV4(ub + 1152 + f * 64); \
      f32x4 hv; \
      _Pragma("unroll") for (int e = 0; e < 4; ++e) { \
        const float xg = sfma(rs, gv[e], sfma(nb, g1[e], g2[e])); \
        const float xu = sfma(rs, uv[e], sfma(nb, u1[e], u2[e])); \
        const float y2 = xg * (-2.302208198f - 0.1029432397f * xg * xg); \
        hv[e] = xg * frcp(1.f + fexp2(y2)) * xu; \
      } \
      hp[f3] = __builtin_bit_cast(u32x2, pack4(hv)); \
      if (f3 >= 1) { \
        const int fa = f3 == 1 ? 0 : 2; \
        const u32x4 wv = widen(hp[fa], hp[f3]); \
        const int pp = p0 + ((gq & 1) ? f3 : fa); \
        if (f3 == 1 || !(gq & 1)) st16_sc1(rhh, (unsigned)(mtile * 43 * 1024 + pp * 512 + (m + 16 * (gq >> 1)) * 16), wv); \
      } \
    } }
  P3_LD(0); P3_LD(1);
  static_for<0, 32>([&](auto GI) __attribute__((always_inline)) NOPK {
    constexpr int gi = decltype(GI)::value;
    (void)sta0; (void)sta1;
    if constexpr (gi == 6) BAR();
    if constexpr (gi == 16) {
      WV2(6, sta0, sta1);
      const f32x4 a = __builtin_bit_cast(f32x4, sta0), bq = __builtin_bit_cast(f32x4, sta1);
      float sm_ = (a[0] + a[2]) + (bq[0] + bq[2]);
      sm_ = xsum32(xsum16(sm_));
      const float mean = sm_ * (1.f / 16.f);
      float d0 = a[0] - mean, d1 = a[2] - mean, d2 = bq[0] - mean, d3 = bq[2] - mean;
      float q = (a[1] + a[3]) + (bq[1] + bq[3]) + 32.f * ((d0 * d0 + d1 * d1) + (d2 * d2 + d3 * d3));
      q = xsum32(xsum16(q));
      rs = 1.f / rbf1(__builtin_sqrtf(rbf1(rbf1(q * (1.f / 512.f)) + 1e-5f)));
      nb = -rbf1(mean) * rs;
      BAR();
      P3_EPI(0, 2 * SLOT);
      FENCE();
    }
    if constexpr (gi == 18) BAR();
    if constexpr (gi == 22) { WVN(4); BAR(); if (threadIdx.x == 0) stflag(flg + F_S5 + r * 16 + j, tag); }
    if constexpr (gi + 2 < 32) { P3_LD(gi + 2); }
    if constexpr (gi < 16 && (gi & 1) == 0) {
      constexpr int ks = gi >> 1;
      if constexpr (ks < 4) { P3_X(ks + 4); }
      if constexpr (ks == 2) { _Pragma("unroll") for (int i = 0; i < 3; ++i) w3_q(w3w, l3w, l16, NF == 5, 5, i); }
      if constexpr (ks == 3) { const unsigned so = (unsigned)(m * 128 + gq * 32); GLD16S(sta0, stb, so, 0); GLD16S(sta1, stb, so, 16); }
      constexpr int pa = gi > 6 ? (gi < 12 ? ((gi - 6) >> 1) * 2 : 6) : 0;
      constexpr int ny = ks == 0 ? 8 : ks == 1 ? 8 : ks == 2 ? 11 : ks == 3 ? 13 : ks == 4 ? 11 + pa : ks == 5 ? 9 + pa : ks == 6 ? 7 + pa : 2 + pa;
      WV2(ny, xa[0][ks], xa[1][ks]);
    }
    FENCE();
    P3_MM(gi);
    FENCE();
    if constexpr (gi >= 6 && gi < 12) { if (kh == (gi & 1)) { constexpr int q = ((gi - 6) >> 1) * 2; w3_q(w3w, l3w, l16, NF == 5, 6 + q / 3, q % 3); w3_q(w3w, l3w, l16, NF == 5, 6 + (q + 1) / 3, (q + 1) % 3); } }
    if constexpr (gi >= 18 && gi < 30) { if (kh == (gi & 1)) { constexpr int q = ((gi - 18) >> 1) * 2; w4_w(w4w, l4w, l16, kh, q); w4_w(w4w, l4w, l16, kh, q + 1); } }
  });
  BAR();
  P3_EPI(1, 0);
  signal(flg + F_S5B + r * 16 + j, tag);
}

__global__ __launch_bounds__(512) __attribute__((amdgpu_waves_per_eu(2, 2))) NOPK
void k1_main(const unsigned short* __restrict__ x, const unsigned short* __restrict__ wqkv, const unsigned short* __restrict__ wphi,
             const unsigned short* __restrict__ wo, const unsigned short* __restrict__ wg, const unsigned short* __restrict__ wu,
             const unsigned short* __restrict__ wd, const unsigned short* __restrict__ cvec, const unsigned short* __restrict__ wmod,
             char* __restrict__ ws, unsigned short* __restrict__ out) {
  extern __shared__ __attribute__((aligned(16))) char sm[];
  const int tid = threadIdx.x, lane = tid & 63, w = __builtin_amdgcn_readfirstlane(tid >> 6);
  const int b = blockIdx.x, xcd = b & 7, idx = b >> 3;
  const int r = xcd * 2 + (idx >> 4), j = idx & 15;
  const int m = lane & 15, gq = lane >> 4;
  const int kh = w >> 2, wr = w & 3;
  u64* flg = (u64*)(ws + WS_FLG);
  const f32x4 z4 = {0, 0, 0, 0};
  const int rh = j >> 3, cb = j & 7;
  const int t0 = j < 6 ? j * 6 : 36 + (j - 6) * 5;
  const bool nf5 = j >= 6;
  const unsigned l16 = lane * 16;
  const char* wt = ws + WS_WT;
  const char* modp = ws + WS_MODP;
  bf16x8 pq[2];
  bf16x8 w2f[8][4];
  f32x4 gav[2], gbv[2], s2v[2];
  const unsigned fpos = (unsigned)((m + 16 * (gq >> 1)) * 16);
  const int n2 = cb * 64 + kh * 32 + gq * 4;
  u64 tag;
  {
    const int g = j, head = 2 * g + kh;
    u32x2 ev; unsigned c2; u32x4 wm[2], xr; unsigned wp0, wp1, wp2, wp3;
    const int bb = b < 192 ? b : b - 192, slab = bb >> 2, kq = bb & 3;
    const unsigned grow = (unsigned)(kq * 128 + w * 16 + (lane >> 3) * 2);
    GLD8S(ev, flg, 0u, 0);
    GLD4(c2, cvec, grow * 2, 0);
    {
      const char* wb = (const char*)(wmod + slab * 64);
      const unsigned wmo = grow * 6144 + (lane & 7) * 16;
      GLD16(wm[0], wb, wmo, 0); GLD16(wm[1], wb + 6144, wmo, 0);
    }
    GLD16(xr, (const char*)(x + (size_t)(r * 128 + j * 8 + w) * 512), l16, 0);
    {
      const char* wpb = (const char*)(wphi + head * 256);
      const unsigned o = (unsigned)(gq * 128 + m * 2);
      GLDU16(wp0, wpb, o, 0); GLDU16(wp1, wpb, o, 32); GLDU16(wp2, wpb, o, 64); GLDU16(wp3, wpb, o, 96);
    }
    __builtin_amdgcn_s_barrier();
    unsigned wu_;
    {
      const char* tb_ = w < 6 ? (const char*)wqkv + (idx * 6 + w) * 8192 : (const char*)wo + (idx * 2 + w - 6) * 8192;
      GLD4(wu_, tb_, (unsigned)(lane * 128), 0);
    }
    WV4(1, c2, wm[0], wm[1], ev); WV4(1, wp0, wp1, wp2, wp3); WV1(1, xr);
    tag = (((u64)__builtin_amdgcn_readfirstlane(ev[1]) << 32) | (u64)(unsigned)__builtin_amdgcn_readfirstlane(ev[0])) + 1ull;
    gemv_part(c2, wm, sm + L_SCR, w, lane);
    BAR();
    if (w == 0) {
      float s = 0;
#pragma unroll
      for (int p = 0; p < 8; ++p) s += ((const float*)(sm + L_SCR))[p * 64 + lane];
      st8_sc1(mkrsrc(ws + WS_MODP, 98304), (unsigned)(kq * 24576 + (slab * 64 + lane) * 8), u32x2{F2U(s), (unsigned)tag});
    }
    const unsigned w1o = (unsigned)((lane >> 2) * 3072 + (((lane & 3) ^ (((lane >> 5) & 1) << 1)) * 16));
    const char* w1w = (const char*)(wqkv + (size_t)(kh * 256 + wr * 16) * 1536 + g * 32);
    const unsigned l1w = (unsigned)(kh * 12288 + wr * 1024);
#define P1_W(c_, seg_) gblk(w1w + ((c_) * 64 * 1536 + (seg_) * 512) * 2, w1o, l1w + w1_off(c_) + (seg_) * 4096)
#pragma unroll
    for (int i = 0; i < 9; ++i) P1_W(i / 3, i % 3);
    float xf[8], rs1, nb1;
    {
#pragma unroll
      for (int i = 0; i < 4; ++i) { xf[2 * i] = lo16(xr[i]); xf[2 * i + 1] = hi16(xr[i]); }
      float s = ((xf[0] + xf[1]) + (xf[2] + xf[3])) + ((xf[4] + xf[5]) + (xf[6] + xf[7]));
      s = xsum2(xsum1(s)); s += dpp<0x124>(s); s += dpp<0x128>(s); s = xsum32(xsum16(s));
      const float mean = s * (1.f / 512.f);
      float q = 0;
#pragma unroll
      for (int i = 0; i < 8; ++i) { const float d = xf[i] - mean; q = sfma(d, d, q); }
      q = xsum2(xsum1(q)); q += dpp<0x124>(q); q += dpp<0x128>(q); q = xsum32(xsum16(q));
      rs1 = 1.f / rbf1(__builtin_sqrtf(rbf1(rbf1(q * (1.f / 512.f)) + 1e-5f)));
      nb1 = rbf1(mean);
    }
    __builtin_amdgcn_s_barrier();
    {
      u32x4 mg[4];
      MODLD(mg, tid * 16);
      WV4(0, mg[0], mg[1], mg[2], mg[3]); WV1(0, wu_);
      const unsigned tl = (unsigned)tag;
      unsigned spins = 0;
      while (true) {
        bool ok = true;
#pragma unroll
        for (int k = 0; k < 4; ++k) ok = ok && mg[k][1] == tl && mg[k][3] == tl;
        if (__builtin_amdgcn_ballot_w64(!ok) == 0ull || ++spins > (1u << 18)) break;
        __builtin_amdgcn_s_sleep(1);
        MODLD(mg, tid * 16);
        WV4(0, mg[0], mg[1], mg[2], mg[3]);
      }
      f32x2 mv = modsum(mg);
      if (tid >= 256) mv = rbf2(mv[0] + 1.f, mv[1] + 1.f);
      *(f32x2*)(sm + L_MOD1 + tid * 8) = mv;
      BAR();
      const char* a_ = sm + L_MOD1 + lane * 32;
      const f32x4 a0 = LDV4(a_), a1 = LDV4(a_ + 16), s0 = LDV4(a_ + 2048), s1 = LDV4(a_ + 2064);
      u32x4 hq;
#pragma unroll
      for (int i = 0; i < 4; ++i) {
        const f32x4 sv = i < 2 ? s0 : s1, av = i < 2 ? a0 : a1;
        f32x2 t2 = rbf2(lo16(xr[i]) - nb1, hi16(xr[i]) - nb1);
        t2 = rbf2(t2[0] * rs1, t2[1] * rs1);
        t2 = rbf2(t2[0] * sv[(i & 1) * 2], t2[1] * sv[(i & 1) * 2 + 1]);
        hq[i] = pack2(t2[0] + av[(i & 1) * 2], t2[1] + av[(i & 1) * 2 + 1]);
      }
      const bf16x8 hv = __builtin_bit_cast(bf16x8, hq);
      *(bf16x8*)(sm + L_HT + lane * 128 + ((w ^ (lane & 7)) * 16)) = hv;
      BAR();
      const int seg = tid >> 3, i8 = tid & 7;
      const u32x4 ho = *(const u32x4*)(sm + L_HT + seg * 128 + ((i8 ^ (seg & 7)) * 16));
      st16_sc1(mkrsrc(ws + WS_H, 2 << 20), (unsigned)(((r * 8 + (j >> 1)) * 16 + (seg >> 2)) * 1024 + ((j & 1) * 8 + i8 + 16 * (seg & 3)) * 16), ho);
    }
    signal(flg + F_S1 + r * 16 + j, tag);
    u32x4 tv[4]; int nvalid, rks; unsigned rdst, rstep, rup; bool risw3;
    const char* rsb; const char* rsb2; unsigned rso; int rpitch;
    {
      int u = w * 256 + b; const bool have = u < 1048; if (!have) u = b;
      const unsigned short* src; int nbk; nvalid = 4; risw3 = u < 704; rup = 0;
      if (risw3) { const int mat = u / 352, v2 = u % 352; rks = v2 / 22; nbk = v2 % 22; src = mat ? wu : wg; rpitch = 1376;
        rdst = WT_W3 + (unsigned)((mat * 86 + nbk * 4) * 16 + rks) * 1024; rstep = 16 * 1024; if (nbk == 21) nvalid = 2;
        rup = WT_UP + (unsigned)((mat * 16 + rks) * 1376 + nbk * 64) * 8; }
      else { const int v = u - 704; rks = v >> 3; nbk = v & 7; src = wd; rpitch = 512; rdst = WT_W4 + (unsigned)(nbk * 4 * 43 + rks) * 1024; rstep = 43 * 1024; }
      int ch = lane & 7; if (nvalid == 2) ch &= 3;
      if (!have) nvalid = 0;
      rsb = (const char*)(src + (size_t)(rks * 32) * rpitch + nbk * 64); rsb2 = rsb + (size_t)32 * rpitch;
      if (!risw3) { rsb = (const char*)(src + (size_t)(pos2ft(2 * rks) * 16) * 512 + nbk * 64); rsb2 = (const char*)(src + (size_t)(pos2ft(2 * rks + 1) * 16) * 512 + nbk * 64); }
      rso = (unsigned)((lane >> 3) * rpitch * 2 + ch * 16);
      if (!have) { rsb = modp; rsb2 = modp; rso = l16; rpitch = 0; }
    }
    if (w < 4) {
#pragma unroll
      for (int i = 0; i < 6; ++i) gblk(w1w + ((3 * 64 + (i / 3) * 256) * 1536 + (i % 3) * 512) * 2, w1o, l1w + w1_off(3) + (i / 3) * 12288 + (i % 3) * 4096);
    }
    u32x4 m2[4], gv[4];
    const int gl = w == 0 ? lane : 0;
    const unsigned gvo = (unsigned)(((gl & 32) ? 2560 : 1024) + cb * 64 + (gl & 31) * 2) * 8;
    MODLD(m2, (unsigned)(1536 * 8 + tid * 16));
    MODLD(gv, gvo);
    waitflags(flg + F_S1 + r * 16, 16, tag, w ^ 7, lane);
    f32x4 acc[3][2][2];
#pragma unroll
    for (int s = 0; s < 3; ++s)
#pragma unroll
      for (int hb = 0; hb < 2; ++hb)
#pragma unroll
        for (int mt = 0; mt < 2; ++mt) acc[s][hb][mt] = z4;
    {
      u32x4 ha[2][8];
      const char* hb0 = ws + WS_H + (size_t)((r * 8 + wr * 2) * 16 + kh * 8) * 1024;
#define P1_H(ks_) { const char* hb_ = hb0 + ((ks_) >> 2) * 4096; GLD16S(ha[0][ks_], hb_, l16, ((ks_) & 3) * 1024); GLD16S(ha[1][ks_], hb_ + 16384, l16, ((ks_) & 3) * 1024); }
#pragma unroll
      for (int ks = 0; ks < 4; ++ks) P1_H(ks);
      const int q = (lane & 15) >> 2, p = lane & 3;
      LB tb[2];
#pragma unroll
      for (int hb = 0; hb < 2; ++hb) tb[hb] = mklb(kh * 12288 + (gq * 8 + q) * 64 + ((((hb ^ kh) * 2 + (p >> 1)) ^ ((gq & 1) << 1)) * 16) + (p & 1) * 8);
      bf16x8 wf[2][6];
#define P1_LD(s_) _Pragma("unroll") for (int i = 0; i < 6; ++i) { constexpr int o_ = w1_off((s_) >> 1) + ((s_) & 1) * 2048; wf[(s_) & 1][i] = cat8(TRB(tb[i & 1], o_ + (i >> 1) * 4096), TRB(tb[i & 1], o_ + (i >> 1) * 4096 + 256)); }
      P1_LD(0);
      FENCE();
      const unsigned w2o = (unsigned)((lane >> 3) * 1024 + (((lane & 7) ^ ((((lane >> 4) & 1) | ((w & 1) << 1)) << 1)) * 16));
#define P1_WO(i_) gblk((const char*)(wo + (size_t)(((i_) * 8 + w) * 8) * 512 + cb * 64), w2o, L_W2 + ((i_) * 8 + w) * 1024)
      static_for<0, 8>([&](auto S) __attribute__((always_inline)) NOPK {
        constexpr int s = decltype(S)::value;
        if constexpr (s == 5) BAR();
        if constexpr (s < 7) { P1_LD(s + 1); }
        if constexpr (s < 4) P1_H(s + 4);
        WV2(s < 4 ? 8 : 2 * (7 - s) + (s > 4 ? ((s - 4) >> 1) * 4 : 0), ha[0][s], ha[1][s]);
        FENCE();
#pragma unroll
        for (int i = 0; i < 6; ++i)
#pragma unroll
          for (int mt = 0; mt < 2; ++mt) {
            const int sg = i >> 1, hb = i & 1;
            if (sg < 2) acc[sg][hb][mt] = mfma32(wf[s & 1][i], __builtin_bit_cast(bf16x8, ha[mt][s]), acc[sg][hb][mt]);
            else acc[sg][hb][mt] = mfma32(__builtin_bit_cast(bf16x8, ha[mt][s]), wf[s & 1][i], acc[sg][hb][mt]);
          }
        FENCE();
        if constexpr (s >= 4) { if (kh == (s & 1)) { constexpr int q0 = ((s - 4) >> 1) * 4; P1_WO(q0); P1_WO(q0 + 1); P1_WO(q0 + 2); P1_WO(q0 + 3); } }
      });
    }
    BAR();
#pragma unroll
    for (int s = 0; s < 3; ++s)
#pragma unroll
      for (int mt = 0; mt < 2; ++mt) *(f32x4*)(sm + (w * 6 + s * 2 + mt) * 1024 + l16) = acc[s][1][mt];
#pragma unroll
    for (int i = 0; i < 4; ++i) GLD16(tv[i], (i < 2 ? rsb : rsb2) + (size_t)(i & 1) * 16 * rpitch, rso, 0);
    BAR();
    f32x4 t[3][2];
#pragma unroll
    for (int s = 0; s < 3; ++s)
#pragma unroll
      for (int mt = 0; mt < 2; ++mt) {
        const f32x4 o = *(const f32x4*)(sm + ((w ^ 4) * 6 + s * 2 + mt) * 1024 + l16);
        t[s][mt] = acc[s][0][mt] + o;
      }
    const bf16x4 wpT = __builtin_bit_cast(bf16x4, u32x2{wp0 | (wp1 << 16), wp2 | (wp3 << 16)});
    bf16x4 ident;
#pragma unroll
    for (int jj = 0; jj < 4; ++jj) ident[jj] = (4 * gq + jj == m) ? (short)0x3f80 : (short)0;
    bf16x4 pkp[2], pkn[2];
#pragma unroll
    for (int mt = 0; mt < 2; ++mt) {
      bf16x4 pos, neg;
      featmap(mfma16(wpT, pack4(t[1][mt]), z4), pos, neg);
      pkp[mt] = pack4(mfma16(pos, ident, z4));
      pkn[mt] = pack4(mfma16(neg, ident, z4));
    }
    const bf16x8 vf = cat8(pack4(t[2][0]), pack4(t[2][1]));
    const bf16x8 fp = cat8(pkp[0], pkp[1]), fn = cat8(pkn[0], pkn[1]);
    const bf16x8 ones = {0x3f80, 0x3f80, 0x3f80, 0x3f80, 0x3f80, 0x3f80, 0x3f80, 0x3f80};
    f32x4 part[4];
    part[0] = mfma32(fp, vf, z4); part[1] = mfma32(fn, vf, z4); part[2] = mfma32(fp, ones, z4); part[3] = mfma32(fn, ones, z4);
    BAR();
#pragma unroll
    for (int i = 0; i < 4; ++i) *(f32x4*)(sm + (w * 4 + i) * 1024 + l16) = part[i];
    BAR();
    {
      const rsrc_t rkv = mkrsrc(ws + WS_KVP, 16 * 32 * KVP_STRIDE);
      f32x4 s = z4;
#pragma unroll
      for (int jj = 0; jj < 4; ++jj) s += *(const f32x4*)(sm + ((kh * 4 + jj) * 4 + wr) * 1024 + l16);
      const unsigned base = (unsigned)((r * 32 + head) * KVP_STRIDE);
      if (wr < 2) st16_sc1(rkv, base + wr * 1024 + l16, __builtin_bit_cast(u32x4, s));
      else if (m == 0) st16_sc1(rkv, base + 2048 + (wr - 2) * 64 + gq * 16, __builtin_bit_cast(u32x4, s));
    }
    signal(flg + F_S2 + g * 16 + r, tag);
    {
      asm volatile("" : "+v"(tv[0]), "+v"(tv[1]), "+v"(tv[2]), "+v"(tv[3]));
      WV4(0, m2[0], m2[1], m2[2], m2[3]); WV4(0, gv[0], gv[1], gv[2], gv[3]);
      {
        const unsigned tl = (unsigned)tag;
        unsigned spins = 0;
        while (true) {
          bool ok = true;
#pragma unroll
          for (int k = 0; k < 4; ++k) ok = ok && m2[k][1] == tl && m2[k][3] == tl && gv[k][1] == tl && gv[k][3] == tl;
          if (__builtin_amdgcn_ballot_w64(!ok) == 0ull || ++spins > (1u << 18)) break;
          __builtin_amdgcn_s_sleep(1);
          MODLD(m2, (unsigned)(1536 * 8 + tid * 16)); MODLD(gv, gvo);
          WV4(0, m2[0], m2[1], m2[2], m2[3]); WV4(0, gv[0], gv[1], gv[2], gv[3]);
        }
      }
      f32x2 mv2 = modsum(m2);
      if (tid >= 256) mv2 = rbf2(mv2[0] + 1.f, mv2[1] + 1.f);
      *(f32x2*)(sm + L_M2V + tid * 8) = mv2;
      if (w == 0) *(f32x2*)(sm + L_GV + lane * 8) = modsum(gv);
      char* lw = sm + w * 4096;
#pragma unroll
      for (int i = 0; i < 4; ++i) {
        const int rl = i * 8 + (lane >> 3);
        const int sw = ((rl >> 1) & 1) | (((rl >> 3) & 1) << 1);
        *(u32x4*)(lw + rl * 128 + (((lane & 7) ^ (sw << 1)) * 16)) = tv[i];
      }
      BAR();
#pragma unroll
      for (int nt = 0; nt < 2; ++nt) { gav[nt] = LDV4(sm + L_GV + (kh * 32 + nt * 16 + gq * 4) * 4); gbv[nt] = LDV4(sm + L_GV + 256 + (kh * 32 + nt * 16 + gq * 4) * 4); s2v[nt] = LDV4(sm + L_M2V + 2048 + (n2 + nt * 16) * 4); }
      if (nvalid > 0) {
        const int q4 = (lane & 15) >> 2, p4 = lane & 3;
        const int sw = (q4 >> 1) | ((gq & 1) << 1);
        const rsrc_t rwt = mkrsrc(ws + WS_WT, 6 << 20);
        bf16x8 f[4];
#pragma unroll
        for (int nt = 0; nt < 4; ++nt) f[nt] = trfrag(lw + (gq * 8 + q4) * 128 + ((nt ^ sw) * 32) + p4 * 8, 512);
        if (risw3) {
          const char* a_ = sm + L_M2V + (rks * 32 + gq * 8) * 4 + ((m & 2) ? 0 : 2048);
          const f32x4 b0 = LDV4(a_), b1 = LDV4(a_ + 16);
          u32x4 hi = {pack2(b0[0], b0[1]), pack2(b0[2], b0[3]), pack2(b1[0], b1[1]), pack2(b1[2], b1[3])};
          u32x4 lo = {pack2(b0[0] - lo16(hi[0]), b0[1] - hi16(hi[0])), pack2(b0[2] - lo16(hi[1]), b0[3] - hi16(hi[1])),
                      pack2(b1[0] - lo16(hi[2]), b1[1] - hi16(hi[2])), pack2(b1[2] - lo16(hi[3]), b1[3] - hi16(hi[3]))};
          const u32x4 zz = {0, 0, 0, 0};
          const bf16x8 sbA = __builtin_bit_cast(bf16x8, m >= 4 ? zz : ((m & 1) ? lo : hi));
#pragma unroll
          for (int nt = 0; nt < 4; ++nt) {
            const f32x4 d = mfma32(sbA, f[nt], z4);
            if (nt < nvalid && gq == 0) st8_sc1(rwt, rup + (unsigned)(nt * 16 + m) * 8, u32x2{F2U(d[0] + d[1]), F2U(d[2] + d[3])});
          }
        }
#pragma unroll
        for (int nt = 0; nt < 4; ++nt) if (nt < nvalid) st16_sc1(rwt, rdst + nt * rstep + l16, __builtin_bit_cast(u32x4, f[nt]));
      }
      WAITV(0);
    }
    waitflags(flg + F_S2 + g * 16, 16, tag, w ^ 7, lane);
    if (tid == 0) stflag(flg + F_RT + b, tag);
    {
      u32x4 pl[4][2]; u32x2 zl;
      const char* kb = ws + WS_KVP + (size_t)((wr * 4) * 32 + head) * KVP_STRIDE;
#pragma unroll
      for (int i = 0; i < 4; ++i) {
        const char* kbi = kb + (size_t)i * 32 * KVP_STRIDE;
        GLD16S(pl[i][0], kbi, l16, 0); GLD16S(pl[i][1], kbi, l16, 1024);
      }
      GLD8S(zl, kb, (unsigned)(gq * 32 * KVP_STRIDE + m * 8), 2048);
#pragma unroll
      for (int mt = 0; mt < 2; ++mt) {
        bf16x4 pos, neg;
        featmap(mfma16(wpT, pack4(t[0][mt]), z4), pos, neg);
        pq[mt] = cat8(pos, neg);
      }
      asm volatile("" : "+v"(pq[0]), "+v"(pq[1]));
      FENCE();
      WV4(0, pl[0][0], pl[0][1], pl[1][0], pl[1][1]); WV4(0, pl[2][0], pl[2][1], pl[3][0], pl[3][1]); WV1(0, zl);
#pragma unroll
      for (int k = 0; k < 2; ++k)
        *(f32x4*)(sm + 65536 + (w * 2 + k) * 1024 + l16) =
            (__builtin_bit_cast(f32x4, pl[0][k]) + __builtin_bit_cast(f32x4, pl[1][k])) + (__builtin_bit_cast(f32x4, pl[2][k]) + __builtin_bit_cast(f32x4, pl[3][k]));
      const float z0 = xsum32(xsum16(U2F(zl[0]))), z1 = xsum32(xsum16(U2F(zl[1])));
      if (gq == 0) *(f32x2*)(sm + w * 128 + m * 8) = f32x2{z0, z1};
    }
    BAR();
    f32x4 tt[4] = {z4, z4, z4, z4};
#pragma unroll
    for (int jj = 0; jj < 4; ++jj) {
      tt[0] += *(const f32x4*)(sm + 65536 + ((kh * 4 + jj) * 2 + 0) * 1024 + l16);
      tt[1] += *(const f32x4*)(sm + 65536 + ((kh * 4 + jj) * 2 + 1) * 1024 + l16);
      tt[2] += *(const f32x4*)(sm + ((kh * 4 + jj) * 2 + 0) * 64 + gq * 16);
      tt[3] += *(const f32x4*)(sm + ((kh * 4 + jj) * 2 + 1) * 64 + gq * 16);
      if (jj == 1) FENCE();
    }
    const bf16x8 kvA = cat8(pack4(tt[0]), pack4(tt[1])), zA = cat8(pack4(tt[2]), pack4(tt[3]));
    const rsrc_t ro = mkrsrc(ws + WS_O, 2 << 20);
    u32x4 rtf;
    GLD16S(rtf, flg + F_RT + w * 32, (unsigned)((lane & 15) * 16), 0);
    u32x2 op[2];
#pragma unroll
    for (int mt = 0; mt < 2; ++mt) {
      f32x4 num = mfma32(kvA, pq[mt], z4), den = mfma32(zA, pq[mt], z4);
      const float inv = frcp(rbf1(rbf1(den[0]) + 1e-6f));
      num = rbf4(num) * inv;
      op[mt] = __builtin_bit_cast(u32x2, pack4(num));
    }
    st16_sc1(ro, (unsigned)(((r * 8 + wr * 2 + (gq & 1)) * 16 + g) * 1024 + kh * 512) + fpos, widen(op[0], op[1]));
    {
      WV1(0, rtf);
      const unsigned tl = (unsigned)tag, th = (unsigned)(tag >> 32);
      const bool ok = rtf[0] == tl && rtf[1] == th && rtf[2] == tl && rtf[3] == th;
      if (__builtin_amdgcn_ballot_w64(!ok) != 0ull) spinflags(flg + F_RT + w * 32, 32, tag, lane);
    }
    signal(flg + F_S3 + r * 16 + j, tag);
  }
  u32x2 x1p[2];
  const int mtile2 = r * 8 + rh * 4 + wr, row2 = mtile2 * 16 + m;
  {
    u32x2 xv[2], yv[2]; float x1v[8];
    float u1 = 0, u2 = 0;
    const char* w3a = w3_base(wt, t0, wr * 2); const char* w3c = w3a + 86 * 16 * 1024;
    if (w >= 4) {
#pragma unroll
      for (int i = 0; i < 12; ++i) w3_q((i % 6) < 3 ? w3a : w3c, (unsigned)(wr * 6144 + ((i % 6) / 3) * 3072), l16, nf5, i / 6, i % 3);
    } else if (w >= 1) {
      const int t = tid - 64, mat = t / 96; int cc = t % 96; if (nf5 && cc >= 80) cc -= 16;
      const unsigned uo = (unsigned)((mat * 16 * 1376 + t0 * 16 + cc) * 8);
      u32x2 uv[16];
#pragma unroll
      for (int k = 0; k < 16; ++k) GLD8S(uv[k], wt + WT_UP + k * 11008, uo, 0);
      WV4(0, uv[0], uv[1], uv[2], uv[3]); WV4(0, uv[4], uv[5], uv[6], uv[7]); WV4(0, uv[8], uv[9], uv[10], uv[11]); WV4(0, uv[12], uv[13], uv[14], uv[15]);
#pragma unroll
      for (int k = 0; k < 16; ++k) { u1 += U2F(uv[k][0]); u2 += U2F(uv[k][1]); }
    }
      {
        const int q4 = (lane & 15) >> 2, p4 = lane & 3;
        const int sw = (q4 >> 1) | ((gq & 1) << 1);
#pragma unroll
        for (int nt = 0; nt < 4; ++nt) {
          const char* fb = sm + L_W2 + kh * 32768 + (gq * 8 + q4) * 128 + ((nt ^ (kh * 2) ^ sw) * 32) + p4 * 8;
#pragma unroll
          for (int s = 0; s < 8; ++s) w2f[s][nt] = trfrag(fb + s * 4096, 512);
        }
      }
    waitflags(flg + F_S3 + r * 16, 16, tag, w, lane);
    u32x4 af[8];
    const char* ob = ws + WS_O + (size_t)(mtile2 * 16 + kh * 8) * 1024;
#pragma unroll
    for (int ks = 0; ks < 4; ++ks) GLD16S(af[ks], ob, l16, ks * 1024);
#pragma unroll
    for (int ks = 4; ks < 8; ++ks) GLD16S(af[ks], ob + 4096, l16, (ks - 4) * 1024);
    GLD8(xv[0], x, (unsigned)(row2 * 1024 + n2 * 2), 0); GLD8(xv[1], x, (unsigned)(row2 * 1024 + n2 * 2), 32);
    f32x4 acc[4] = {z4, z4, z4, z4};
#pragma unroll
    for (int ks = 0; ks < 8; ++ks) {
      WV1(7 - ks + 2, af[ks]);
      FENCE();
#pragma unroll
      for (int nt = 0; nt < 4; ++nt) acc[nt] = mfma32(w2f[ks][nt], __builtin_bit_cast(bf16x8, af[ks]), acc[nt]);
      FENCE();
    }
#pragma unroll
    for (int nt = 0; nt < 2; ++nt) *(f32x4*)(sm + L_TOP + (w * 2 + nt) * 1024 + l16) = acc[2 + nt];
    WV2(0, xv[0], xv[1]);
#pragma unroll
    for (int i = 0; i < 3; ++i) w3_q(w3_base(wt, t0, w), (unsigned)w * 3072, l16, nf5, 2, i);
    BAR();
    const rsrc_t rx1 = mkrsrc(ws + WS_X1, 2 << 20), rst = mkrsrc(ws + WS_ST2, 256 << 10);
#pragma unroll
    for (int nt = 0; nt < 2; ++nt) {
      const f32x4 a = acc[nt] + *(const f32x4*)(sm + L_TOP + ((w ^ 4) * 2 + nt) * 1024 + l16);
      const f32x4 ga4 = rbf4(gav[nt] * rbf4(a));
      x1p[nt] = u32x2{pack2(lo16(xv[nt][0]) + ga4[0], hi16(xv[nt][0]) + ga4[1]), pack2(lo16(xv[nt][1]) + ga4[2], hi16(xv[nt][1]) + ga4[3])};
      x1v[nt * 4 + 0] = lo16(x1p[nt][0]); x1v[nt * 4 + 1] = hi16(x1p[nt][0]); x1v[nt * 4 + 2] = lo16(x1p[nt][1]); x1v[nt * 4 + 3] = hi16(x1p[nt][1]);
      yv[nt] = u32x2{pack2(x1v[nt * 4] * s2v[nt][0], x1v[nt * 4 + 1] * s2v[nt][1]), pack2(x1v[nt * 4 + 2] * s2v[nt][2], x1v[nt * 4 + 3] * s2v[nt][3])};
    }
    st16_sc1(rx1, (unsigned)((mtile2 * 16 + cb * 2 + kh) * 1024 + (gq & 1) * 512) + fpos, widen(yv[0], yv[1]));
    float s = ((x1v[0] + x1v[1]) + (x1v[2] + x1v[3])) + ((x1v[4] + x1v[5]) + (x1v[6] + x1v[7]));
    s = xsum32(xsum16(s));
    const float mean = s * (1.f / 32.f);
    float q2 = 0;
#pragma unroll
    for (int jj = 0; jj < 8; ++jj) { float d = x1v[jj] - mean; q2 += d * d; }
    q2 = xsum32(xsum16(q2));
    if (gq == 0) { f32x2 st = {mean, q2}; st8_sc1(rst, (unsigned)(row2 * 128 + (cb * 2 + kh) * 8), __builtin_bit_cast(u32x2, st)); }
    signal(flg + F_S4 + r * 16 + j, tag);
    if (w >= 4) {
#pragma unroll
      for (int i = 0; i < 12; ++i) w3_q((i % 6) < 3 ? w3a : w3c, (unsigned)(wr * 6144 + ((i % 6) / 3) * 3072), l16, nf5, 3 + i / 6, i % 3);
    } else if (w >= 1) {
      const int t = tid - 64;
      ((float*)(sm + L_TOP))[t] = u1; ((float*)(sm + L_TOP + 768))[t] = u2;
    }
    waitflags(flg + F_S4 + r * 16, 16, tag, w, lane);
  }
  if (j < 6) phase3<6>(sm, ws, r, j, t0, cb, w, lane, tag);
  else phase3<5>(sm, ws, r, j, t0, cb, w, lane, tag);
  {
    const char* w4a = w4_base(wt, cb, wr * 2);
    if (w >= 4) {
#pragma unroll
      for (int i = 12; i < 20; ++i) { w4_w(w4a, (unsigned)wr * 2048, l16, wr >> 1, i); w4_w(w4a + 43 * 1024, (unsigned)wr * 2048 + 1024, l16, wr >> 1, i); }
    }
    const LB fb = mklb(kh * 4096 + l16);
    bf16x8 wf[4][4];
#pragma unroll
    for (int s = 0; s < 3; ++s)
#pragma unroll
      for (int nt = 0; nt < 4; ++nt) wf[s][nt] = LDB(fb, w4_off(s) + nt * 1024);
    waitflags(flg + F_S5 + r * 16, 16, tag, w, lane);
    u32x4 af[22];
    const char* hb0 = ws + WS_HH + (size_t)(mtile2 * 43 + kh * 12) * 1024;
    const char* hb1 = ws + WS_HH + (size_t)(mtile2 * 43 + 24 + kh * 10) * 1024;
    const unsigned l21 = l16 + (kh ? 0u : 1024u);
#define P4_A(i_) { if ((i_) < 12) { GLD16S(af[i_], hb0 + ((i_) >> 2) * 4096, l16, ((i_) & 3) * 1024); } else if ((i_) < 21) { GLD16S(af[i_], hb1 + (((i_) - 12) >> 2) * 4096, l16, (((i_) - 12) & 3) * 1024); } else { GLD16S(af[21], hb1 + 8 * 1024, l21, 0); } }
    { const char* w4w = w4_base(wt, cb, w); w4_w(w4w, (unsigned)w * 1024, l16, kh, 20); w4_w(w4w, (unsigned)w * 1024, l16, kh, 21); }
#pragma unroll
    for (int i = 0; i < 8; ++i) P4_A(i);
    f32x4 acc[4] = {z4, z4, z4, z4};
#pragma unroll
    for (int i = 0; i < 22; ++i) {
      if (i == 6) {
        waitflags(flg + F_S5B + r * 16, 16, tag, w, lane);
#pragma unroll
        for (int k = 12; k < 18; ++k) P4_A(k);
      }
      if (i + 3 < 22) {
#pragma unroll
        for (int nt = 0; nt < 4; ++nt) wf[(i + 3) & 3][nt] = LDB(fb, w4_off(i + 3) + nt * 1024);
      }
      if (i < 4) P4_A(i + 8);
      if (i >= 8 && i < 12) P4_A(i + 10);
      WV1(i < 4 ? 8 : i < 6 ? 11 - i : i < 8 ? 17 - i : i < 12 ? 10 : 21 - i, af[i]);
      FENCE();
      if (i < 21 || kh == 0) {
#pragma unroll
        for (int nt = 0; nt < 4; ++nt) acc[nt] = mfma32(wf[i & 3][nt], __builtin_bit_cast(bf16x8, af[i]), acc[nt]);
      }
      FENCE();
    }
    BAR();
#pragma unroll
    for (int nt = 0; nt < 2; ++nt) *(f32x4*)(sm + (w * 2 + nt) * 1024 + l16) = acc[2 + nt];
    BAR();
    u32x2 ov[2];
#pragma unroll
    for (int nt = 0; nt < 2; ++nt) {
      const f32x4 a = acc[nt] + *(const f32x4*)(sm + ((w ^ 4) * 2 + nt) * 1024 + l16);
      const f32x4 gb4 = rbf4(gbv[nt] * rbf4(a));
      ov[nt] = u32x2{pack2(lo16(x1p[nt][0]) + gb4[0], hi16(x1p[nt][0]) + gb4[1]), pack2(lo16(x1p[nt][1]) + gb4[2], hi16(x1p[nt][1]) + gb4[3])};
    }
    *(u32x4*)(out + (size_t)row2 * 512 + cb * 64 + kh * 32 + (gq & 1) * 16 + (gq >> 1) * 8) = widen(ov[0], ov[1]);
    if (b == 0 && tid == 0) stflag(flg + F_E, tag);
  }
}

extern "C" void kernel_launch(void* const* d_in, const int* in_sizes, int n_in, void* d_out, int out_size, void* d_ws, size_t ws_size,
                              hipStream_t stream) {
  const unsigned short* x = (const unsigned short*)d_in[0];
  const unsigned short* wqkv = (const unsigned short*)d_in[1];
  const unsigned short* wphi = (const unsigned short*)d_in[2];
  const unsigned short* wo = (const unsigned short*)d_in[3];
  const unsigned short* wg = (const unsigned short*)d_in[5];
  const unsigned short* wu = (const unsigned short*)d_in[6];
  const unsigned short* wd = (const unsigned short*)d_in[7];
  const unsigned short* c = (const unsigned short*)d_in[8];
  const unsigned short* wm = (const unsigned short*)d_in[9];
  char* ws = (char*)d_ws;
  (void)hipFuncSetAttribute((const void*)k1_main, hipFuncAttributeMaxDynamicSharedMemorySize, 163840);
  k1_main<<<256, 512, 163840, stream>>>(x, wqkv, wphi, wo, wg, wu, wd, c, wm, ws, (unsigned short*)d_out);
}
```
